```python
import torch

def _softmax(x, axis=-1):
    return torch.softmax(x, dim=axis)
def _gelu(x):
    return torch.nn.functional.gelu(x, approximate="tanh")
def _silu(x):
    return torch.nn.functional.silu(x)
def _sigmoid(x):
    return torch.sigmoid(x)
def _layernorm(x, axis=-1, eps=1e-5):
    m = x.mean(dim=axis, keepdim=True)
    v = x.var(dim=axis, keepdim=True, unbiased=False)
    return (x - m) / torch.sqrt(v + eps)
def _scatter_add(tgt, idx, src, axis=0):
    return tgt.clone().index_add_(axis, idx, src)
def _segment_sum(src, seg, n):
    out = torch.zeros((n,) + src.shape[1:], dtype=src.dtype)
    return out.index_add_(0, seg, src)
def _pad(x, pw):
    flat = tuple(v for pair in reversed(pw) for v in pair)
    return torch.nn.functional.pad(x, flat)

kernel_name = 'recurrent_scan__mha_block_cached__geglu_ffn__prompt'

def setup_inputs():

    _w = (lambda a: (lambda t: t.to(torch.bfloat16) if t.is_floating_point() else t)(torch.as_tensor(a)))
    def _A_setup():
        B, Sq, D = 1, 2048, 576
        H, Sk, Dh = 32, 2048, 128
        return {
            "x": _w((lambda *s: torch.randn(*s, dtype=torch.bfloat16) * 0.05)(B, Sq, D)),
            "k_cache": _w((lambda *s: torch.randn(*s, dtype=torch.bfloat16) * 0.8)(B, H, Sk, Dh)),
            "v_cache": _w((lambda *s: torch.randn(*s, dtype=torch.bfloat16) * 0.2)(B, H, Sk, Dh)),
            "wq": _w((lambda *s: torch.randn(*s, dtype=torch.bfloat16) * 0.8)(D, H * Dh) * (D ** -0.5)),
            "wo": _w((lambda *s: torch.randn(*s, dtype=torch.bfloat16) * 0.2)(H * Dh, D) * ((H * Dh) ** -0.5)),
        }
    _A_in = _A_setup()

    def _B_setup():
        B, S, D, F = 1, 2048, 576, 4608
        return {
            "x": _w((lambda *s: torch.randn(*s, dtype=torch.bfloat16) * 0.05)(B, S, D)),
            "w_gate": _w((lambda *s: torch.randn(*s, dtype=torch.bfloat16) * 0.1)(D, F)),
            "w_up": _w((lambda *s: torch.randn(*s, dtype=torch.bfloat16) * 0.1)(D, F)),
            "w_down": _w((lambda *s: torch.randn(*s, dtype=torch.bfloat16) * 0.1)(F, D)),
        }
    _B_in = _B_setup()

    return {
        "A_x": _A_in["x"],
        "A_k_cache": _A_in["k_cache"],
        "A_v_cache": _A_in["v_cache"],
        "A_wq": _A_in["wq"],
        "A_wo": _A_in["wo"],
        "B_x": _B_in["x"],
        "B_w_gate": _B_in["w_gate"],
        "B_w_up": _B_in["w_up"],
        "B_w_down": _B_in["w_down"],
    }

def reference(A_x, A_k_cache, A_v_cache, A_wq, A_wo, B_x, B_w_gate, B_w_up, B_w_down):
    def _A_ref(x, k_cache, v_cache, wq, wo):
        B, Sq, D = x.shape
        H, Dh = 32, 128
        q = (lambda x, *d: x.permute(*d))((x @ wq).reshape(B, Sq, H, Dh), 0, 2, 1, 3)
        scores = torch.einsum("bhqd,bhkd->bhqk", q, k_cache) * (Dh ** -0.5)
        p = _softmax(scores, axis=-1)
        o = torch.einsum("bhqk,bhkd->bhqd", p, v_cache)
        return (lambda x, *d: x.permute(*d))(o, 0, 2, 1, 3).reshape(B, Sq, H * Dh) @ wo

    def _B_ref(x, w_gate, w_up, w_down):
        return (_gelu(x @ w_gate) * (x @ w_up)) @ w_down

    x = A_x
    xn = _layernorm(x, axis=-1)
    s = _A_ref(xn, A_k_cache, A_v_cache, A_wq, A_wo)
    g = _silu(_B_ref(xn, B_w_gate, B_w_up, B_w_down))
    return x + s * g
```

```cpp
#include <hip/hip_runtime.h>
#include <hip/hip_bf16.h>
#include <utility>

typedef float    f32x16 __attribute__((ext_vector_type(16)));
typedef float    f32x2  __attribute__((ext_vector_type(2)));
typedef float    f32x4  __attribute__((ext_vector_type(4)));
typedef __bf16   bf16x8 __attribute__((ext_vector_type(8)));
typedef __bf16   bf16x4 __attribute__((ext_vector_type(4)));
typedef __bf16   bf16x2 __attribute__((ext_vector_type(2)));
typedef unsigned u32x4  __attribute__((ext_vector_type(4)));
typedef unsigned u32x2  __attribute__((ext_vector_type(2)));
template <int... Is> using iseq = std::integer_sequence<int, Is...>;
template <int N> using make_iseq = std::make_integer_sequence<int, N>;
typedef __amdgpu_buffer_rsrc_t srd_t;

constexpr int SEQ = 2048, DM = 576, NH = 32, DH = 128, FF = 4608;
constexpr size_t WS_XN = 0;
constexpr size_t WS_H = 4u << 20;
constexpr size_t WS_O = 24u << 20;
constexpr size_t WS_SLAB = 48u << 20;
constexpr size_t WS_FLAG = 72u << 20;

#define SRD_WORD3 0x00020000
#define DI __device__ __forceinline__
#define GAP() __builtin_amdgcn_sched_barrier(0)
#define ACLOB_LO "a0","a1","a2","a3","a4","a5","a6","a7","a8","a9","a10","a11","a12","a13","a14","a15","a16","a17","a18","a19","a20","a21","a22","a23","a24","a25","a26","a27","a28","a29","a30","a31","a32","a33","a34","a35","a36","a37","a38","a39","a40","a41","a42","a43","a44","a45","a46","a47","a48","a49","a50","a51","a52","a53","a54","a55","a56","a57","a58","a59","a60","a61","a62","a63","a64","a65","a66","a67","a68","a69","a70","a71","a72","a73","a74","a75","a76","a77","a78","a79","a80","a81","a82","a83","a84","a85","a86","a87","a88","a89","a90","a91","a92","a93","a94","a95","a96","a97","a98","a99","a100","a101","a102","a103","a104","a105","a106","a107","a108","a109","a110","a111","a112","a113","a114","a115","a116","a117","a118","a119","a120","a121","a122","a123","a124","a125","a126","a127"
#define ACLOB_HI "a128","a129","a130","a131","a132","a133","a134","a135","a136","a137","a138","a139","a140","a141","a142","a143","a144","a145","a146","a147","a148","a149","a150","a151","a152","a153","a154","a155","a156","a157","a158","a159","a160","a161","a162","a163","a164","a165","a166","a167","a168","a169","a170","a171","a172","a173","a174","a175","a176","a177","a178","a179","a180","a181","a182","a183","a184","a185","a186","a187","a188","a189","a190","a191","a192","a193","a194","a195","a196","a197","a198","a199","a200","a201","a202","a203","a204","a205","a206","a207","a208","a209","a210","a211","a212","a213","a214","a215","a216","a217","a218","a219","a220","a221","a222","a223","a224","a225","a226","a227","a228","a229","a230","a231","a232","a233","a234","a235","a236","a237","a238","a239","a240","a241","a242","a243","a244","a245","a246","a247","a248","a249","a250","a251","a252","a253","a254","a255"

DI unsigned rfl(unsigned x) { return __builtin_amdgcn_readfirstlane(x); }
DI unsigned cvtpk(float lo, float hi) { f32x2 v = {lo, hi}; bf16x2 b = __builtin_convertvector(v, bf16x2); return __builtin_bit_cast(unsigned, b); }
DI float max3f(float a, float b, float c) { float r; asm("v_max3_f32 %0, %1, %2, %3" : "=v"(r) : "v"(a), "v"(b), "v"(c)); return r; }
DI float max2f(float a, float b) { float r; asm("v_max_f32_e32 %0, %1, %2" : "=v"(r) : "v"(a), "v"(b)); return r; }
DI float fadd_s(float a, float b) { float r; asm("v_add_f32_e32 %0, %1, %2" : "=v"(r) : "v"(a), "v"(b)); return r; }
DI float fmul_s(float a, float b) { float r; asm("v_mul_f32_e32 %0, %1, %2" : "=v"(r) : "v"(a), "v"(b)); return r; }
DI float fsub_s(float a, float b) { float r; asm("v_sub_f32_e32 %0, %1, %2" : "=v"(r) : "v"(a), "v"(b)); return r; }
DI float ffma_s(float a, float b, float c) { float r; asm("v_fma_f32 %0, %1, %2, %3" : "=v"(r) : "v"(a), "v"(b), "v"(c)); return r; }
DI srd_t mk_srd(const void* p, unsigned bytes) { return __builtin_amdgcn_make_buffer_rsrc((void*)p, (short)0, (int)bytes, SRD_WORD3); }
template <int R> DI float acc_rd() { float r; asm volatile("v_accvgpr_read_b32 %0, a[%c1]" : "=v"(r) : "i"(R)); return r; }
template <int R> DI void acc_wr(unsigned w) { asm volatile("v_accvgpr_write_b32 a[%c1], %0" :: "v"(w), "i"(R) : ACLOB_LO, ACLOB_HI); }
template <int N> DI void wait_vm() { asm volatile("s_waitcnt vmcnt(%c0)" :: "i"(N) : "memory"); }
DI void dma16(unsigned m0v, unsigned voff, srd_t srd, unsigned soff) {
  asm volatile("s_mov_b32 m0, %0\n\ts_nop 0\n\tbuffer_load_dwordx4 %1, %2, %3 offen lds" :: "s"(m0v), "v"(voff), "s"(srd), "s"(soff) : "m0", "memory");
}

__global__ __launch_bounds__(256) void k1_layernorm(const unsigned short* __restrict__ x, unsigned short* __restrict__ xn, unsigned* __restrict__ flags) {
  __shared__ __attribute__((aligned(16))) unsigned long long stg[72 * 16];
  const unsigned tid = threadIdx.x, l = tid & 31u, tok = tid >> 5;
  if (blockIdx.x == 0) flags[tid] = 0u;
  const unsigned t = blockIdx.x * 8u + tok;
  const u32x4* row = (const u32x4*)(x + (size_t)t * DM);
  u32x4 v[3];
  v[0] = row[l]; v[1] = row[l + 32]; v[2] = (l < 8u) ? row[l + 64] : (u32x4){0u, 0u, 0u, 0u};
  float f[3][8]; float s = 0.f;
  #pragma unroll
  for (int c = 0; c < 3; ++c)
    #pragma unroll
    for (int e = 0; e < 4; ++e) { f[c][2 * e] = __uint_as_float(v[c][e] << 16); f[c][2 * e + 1] = __uint_as_float(v[c][e] & 0xffff0000u); s += f[c][2 * e] + f[c][2 * e + 1]; }
  #pragma unroll
  for (int m = 16; m >= 1; m >>= 1) s += __shfl_xor(s, m, 32);
  const float mean = s * (1.f / DM);
  float q = 0.f;
  #pragma unroll
  for (int c = 0; c < 3; ++c)
    #pragma unroll
    for (int e = 0; e < 8; ++e) { const float d = f[c][e] - mean; f[c][e] = d; if (c < 2 || l < 8u) q += d * d; }
  #pragma unroll
  for (int m = 16; m >= 1; m >>= 1) q += __shfl_xor(q, m, 32);
  const float rstd = 1.f / sqrtf(q * (1.f / DM) + 1e-5f);
  #pragma unroll
  for (int c = 0; c < 3; ++c) {
    if (c < 2 || l < 8u) {
      const unsigned m = l + 32u * c, sstep = m >> 1, jj = m & 1u;
      const unsigned long long lo = (unsigned long long)cvtpk(f[c][0] * rstd, f[c][1] * rstd) | ((unsigned long long)cvtpk(f[c][2] * rstd, f[c][3] * rstd) << 32);
      const unsigned long long hi = (unsigned long long)cvtpk(f[c][4] * rstd, f[c][5] * rstd) | ((unsigned long long)cvtpk(f[c][6] * rstd, f[c][7] * rstd) << 32);
      stg[(2u * sstep) * 16u + tok * 2u + jj] = lo;
      stg[(2u * sstep + 1u) * 16u + tok * 2u + jj] = hi;
    }
  }
  __syncthreads();
  const unsigned tb = blockIdx.x >> 2, tbase = (blockIdx.x & 3u) * 8u;
  for (unsigned u = tid; u < 576u; u += 256u) {
    const unsigned line = u >> 3, tk = u & 7u;
    const u32x4 w = *(const u32x4*)&stg[line * 16u + tk * 2u];
    *(u32x4*)((char*)xn + ((size_t)(tb * 36u + (line >> 1)) * 1024u + (line & 1u) * 512u + (tbase + tk) * 16u)) = w;
  }
}

#define KRING 0x0000u
#define VRING 0x8000u
#define WRING 0x10000u
constexpr unsigned WSLOT = 20480u;
constexpr size_t SHM_K2 = 0x10000 + 3 * 20480;
constexpr unsigned KRS = 256u, KTILEB = 64u * KRS;
constexpr int NT_KV = SEQ / 64;
constexpr float THR = 8.0f;

constexpr int g_nt(int gc) { return gc / 9 == 1 ? 5 : 4; }
constexpr int g_ngaps(int gc) { return 8 * g_nt(gc); }
constexpr int EPI_OPS = 21;
constexpr int epi_units(int ph) { return ph == 1 ? 32 : (ph == 2 ? 40 : 0); }
constexpr int epi_lo(int ph, int G) { const int tg = 9 * 8 * (ph == 1 ? 5 : 4), to = epi_units(ph) * EPI_OPS; return G >= tg ? to : (int)(((long)G * to + tg - 1) / tg); }
constexpr int epi_stores(int gc, int M) {
  const int ph = gc / 9; if (ph == 0) return 0;
  const int G = (gc % 9) * g_ngaps(gc) + M; int n = 0;
  for (int o = epi_lo(ph, G); o < epi_lo(ph, G + 1); ++o) if (o % EPI_OPS == EPI_OPS - 1 && (o / EPI_OPS) % 4 == 3) ++n;
  return n;
}
constexpr int g_npiece(int gc) { return gc > 26 ? 0 : (gc / 9 == 1 ? 5 : 4); }
constexpr int g_xload(int gc, int M) { const int nt = g_nt(gc), ks = M / (2 * nt), i = M % (2 * nt); return (gc + 1 <= 26 && ks < 2 && i >= nt && i < nt + 4) ? 4 * ks + (i - nt) : -1; }
constexpr int g_wpiece(int gc, int M) { const int nt = g_nt(gc), ks = M / (2 * nt), i = M % (2 * nt), p = g_npiece(gc + 2); return (p > 0 && ks == 2 && i >= 2 * nt - p) ? i - (2 * nt - p) : -1; }
constexpr int g_kvpiece(int gc, int M) {
  const int ks = M / 8, i = M % 8;
  if (gc == 25 && ks == 2 && i >= 4) return i - 4;
  if (gc == 26 && ks < 2 && i >= 4) return 4 + 4 * ks + (i - 4);
  return -1;
}
constexpr int g_nvm(int gc, int M) { return (g_xload(gc, M) >= 0) + (g_wpiece(gc, M) >= 0) + (g_kvpiece(gc, M) >= 0) + epi_stores(gc, M); }
constexpr int g_vm_range(int gc, int m0, int m1) { int n = 0; for (int m = m0; m < m1; ++m) n += g_nvm(gc, m); return n; }
constexpr int g_wait_bar(int gc) { const int nt = g_nt(gc); return g_vm_range(gc, 0, 6 * nt) + (gc > 0 ? g_vm_range(gc - 1, 6 * g_nt(gc - 1), 8 * g_nt(gc - 1)) : 0); }
constexpr int g_wait_end(int gc) { const int nt = g_nt(gc); return g_vm_range(gc, 2 * nt + nt + 4, 8 * nt); }

struct GC {
  unsigned trb[3];
  unsigned wsrc1, wsrc2[5], wsrc3, xvo[2], hvo;
  srd_t srdX, srdGU, srdWq, srdH, srdK, srdV;
  unsigned m0g1, m0g2, m0q, sof1, sof2, sofq, hso;
  unsigned vksrc[2], vvsrc, m0k, m0v, sofk, sofv;
  bf16x8 B[2][4][2];
  bf16x4 alo[2][5], aup[2][5];
  bf16x8 A[2][5];
  f32x16 accV[2];
  float eg[2], eu[2], et[2], egu[2], kc0, kc1; u32x4 hw;
};

template <int PH, int T, int R> DI float epi_src(GC& c) {
  if constexpr (PH == 1) return acc_rd<16 * T + R>();
  else if constexpr (T < 8) return acc_rd<128 + 16 * T + R>();
  else return c.accV[T - 8][R];
}
template <int PH, int O> DI void epi_op(GC& c) {
  constexpr int u = O / EPI_OPS, o = O % EPI_OPS, T = u >> 2, pr = u & 3;
  if constexpr (o == 0) c.eg[0] = epi_src<PH, T, 2 * pr>(c);
  if constexpr (o == 1) c.eg[1] = epi_src<PH, T, 2 * pr + 1>(c);
  if constexpr (o == 2) c.eu[0] = epi_src<PH, T, 8 + 2 * pr>(c);
  if constexpr (o == 3) c.eu[1] = epi_src<PH, T, 8 + 2 * pr + 1>(c);
  if constexpr (o == 4 || o == 5)   asm volatile("v_mul_f32_e32 %0, %1, %1" : "=v"(c.et[o - 4]) : "v"(c.eg[o - 4]));
  if constexpr (o == 6 || o == 7)   asm volatile("v_fma_f32 %0, %0, %1, %2" : "+v"(c.et[o - 6]) : "v"(c.kc1), "v"(c.kc0));
  if constexpr (o == 8 || o == 9)   asm volatile("v_mul_f32_e32 %0, %0, %1" : "+v"(c.et[o - 8]) : "v"(c.eg[o - 8]));
  if constexpr (o == 10)            asm volatile("v_exp_f32_e32 %0, %0" : "+v"(c.et[0]));
  if constexpr (o == 11)            asm volatile("v_mul_f32_e32 %0, %1, %2" : "=v"(c.egu[0]) : "v"(c.eg[0]), "v"(c.eu[0]));
  if constexpr (o == 12)            asm volatile("v_exp_f32_e32 %0, %0" : "+v"(c.et[1]));
  if constexpr (o == 13)            asm volatile("v_mul_f32_e32 %0, %1, %2" : "=v"(c.egu[1]) : "v"(c.eg[1]), "v"(c.eu[1]));
  if constexpr (o == 14 || o == 15) asm volatile("v_add_f32_e32 %0, 1.0, %0" : "+v"(c.et[o - 14]));
  if constexpr (o == 16 || o == 17) asm volatile("v_rcp_f32_e32 %0, %0" : "+v"(c.et[o - 16]));
  if constexpr (o == 18 || o == 19) asm volatile("v_mul_f32_e32 %0, %0, %1" : "+v"(c.egu[o - 18]) : "v"(c.et[o - 18]));
  if constexpr (o == 20) {
    unsigned w; asm volatile("v_cvt_pk_bf16_f32 %0, %1, %2" : "=v"(w) : "v"(c.egu[0]), "v"(c.egu[1]));
    c.hw[pr] = w;
    if constexpr (pr == 3) {
      constexpr int ctg = (PH == 1 ? 0 : 4) + (T >> 1), qb = T & 1;
      __builtin_amdgcn_raw_buffer_store_b128(c.hw, c.srdH, c.hvo, c.hso + (unsigned)(qb * 288 + ctg) * 1024u, 0);
    }
  }
}
template <int PH, int O0, int... Is> DI void epi_ops(GC& c, iseq<Is...>) { (epi_op<PH, O0 + Is>(c), ...); }

template <int GCI, int KS, int CT, int HALF> DI void g_trread(GC& c) {
  constexpr int ph = GCI / 9, nt = g_nt(GCI), slot = GCI % 3, par = KS & 1;
  constexpr int off = slot * (int)WSLOT + (ph == 2 ? 4096 * KS + 2048 * HALF + 512 * CT : ((2 * KS + HALF) * nt + CT) * 256);
  if constexpr (HALF == 0) asm volatile("ds_read_b64_tr_b16 %0, %1 offset:%c2" : "=v"(c.alo[par][CT]) : "v"(c.trb[ph]), "i"(off) : "memory");
  else                     asm volatile("ds_read_b64_tr_b16 %0, %1 offset:%c2" : "=v"(c.aup[par][CT]) : "v"(c.trb[ph]), "i"(off) : "memory");
}
template <int GCI, int X> DI void g_xld(GC& c) {
  constexpr int ks = X >> 1, qb = X & 1;
  asm volatile("buffer_load_dwordx4 %0, %1, %2, %3 offen offset:%c4" : "=v"(c.B[GCI & 1][ks][qb]) : "v"(c.xvo[qb]), "s"(c.srdX), "s"((unsigned)((GCI % 9) * 4096)), "i"(ks * 1024) : "memory");
}
template <int GCI, int P> DI void g_wdma(GC& c, unsigned ldsw) {
  constexpr int ph = GCI / 9, cc = GCI % 9; constexpr unsigned slot = (GCI % 3) * WSLOT;
  if constexpr (ph == 0)      dma16(c.m0g1 + slot + 1024u * P, c.wsrc1, c.srdGU, c.sof1 + (unsigned)(cc * 64 + 8 * P) * (FF * 2u));
  else if constexpr (ph == 1) dma16(c.m0g2 + slot + 1024u * P, c.wsrc2[P], c.srdGU, c.sof2 + (unsigned)(cc * 64) * (FF * 2u));
  else                        dma16(c.m0q + slot + 1024u * P, c.wsrc3, c.srdWq, c.sofq + (unsigned)(cc * 64 + 8 * (P >> 1)) * 8192u + 128u * (P & 1));
}
template <int P> DI void g_kvdma(GC& c) {
  constexpr int q = P & 3;
  if constexpr (P < 4)      dma16(c.m0k + 1024u * q, c.vksrc[q & 1], c.srdK, c.sofk + KRS * 4u * q);
  else if constexpr (P < 8) dma16(c.m0v + 1024u * q, c.vvsrc, c.srdV, c.sofv + KRS * 8u * (q >> 1) + 128u * (q & 1));
  else                      dma16(c.m0k + 0x4000u + 1024u * q, c.vksrc[q & 1], c.srdK, KTILEB + c.sofk + KRS * 4u * q);
}
template <int GCI, int M> DI void g_gap(GC& c) {
  constexpr int ph = GCI / 9, cc = GCI % 9, nt = g_nt(GCI), ks = M / (2 * nt), i = M % (2 * nt), ct = i >> 1, qb = i & 1;
  constexpr bool Z = (cc == 0 && ks == 0);
  if constexpr (ph == 1 && ct == 4) {
    if constexpr (Z) asm volatile("v_mfma_f32_32x32x16_bf16 %0, %1, %2, 0" : "=&v"(c.accV[qb]) : "v"(c.A[ks & 1][ct]), "v"(c.B[GCI & 1][ks][qb]));
    else             asm volatile("v_mfma_f32_32x32x16_bf16 %0, %1, %2, %0" : "+v"(c.accV[qb]) : "v"(c.A[ks & 1][ct]), "v"(c.B[GCI & 1][ks][qb]));
  } else {
    constexpr int ao = (ph == 1 ? 128 : 0) + 16 * (2 * ct + qb);
    if constexpr (Z) asm volatile("v_mfma_f32_32x32x16_bf16 a[%c2:%c3], %0, %1, 0" :: "v"(c.A[ks & 1][ct]), "v"(c.B[GCI & 1][ks][qb]), "i"(ao), "i"(ao + 15) : ACLOB_LO, ACLOB_HI);
    else             asm volatile("v_mfma_f32_32x32x16_bf16 a[%c2:%c3], %0, %1, a[%c2:%c3]" :: "v"(c.A[ks & 1][ct]), "v"(c.B[GCI & 1][ks][qb]), "i"(ao), "i"(ao + 15) : ACLOB_LO, ACLOB_HI);
  }
  constexpr int ngc = ks == 3 ? GCI + 1 : GCI, nks = (ks + 1) & 3;
  if constexpr (ngc <= 26 && i < g_nt(ngc)) { g_trread<ngc, nks, i, 0>(c); g_trread<ngc, nks, i, 1>(c); }
  if constexpr (g_xload(GCI, M) >= 0) g_xld<GCI + 1, g_xload(GCI, M)>(c);
  if constexpr (g_wpiece(GCI, M) >= 0) g_wdma<GCI + 2, g_wpiece(GCI, M)>(c, 0u);
  if constexpr (g_kvpiece(GCI, M) >= 0) g_kvdma<g_kvpiece(GCI, M)>(c);
  if constexpr (ph > 0) { constexpr int G = cc * g_ngaps(GCI) + M, o0 = epi_lo(ph, G), o1 = epi_lo(ph, G + 1); epi_ops<ph, o0>(c, make_iseq<o1 - o0>{}); }
  GAP();
}
#define G_A5(p) "+v"(c.alo[p][0]), "+v"(c.alo[p][1]), "+v"(c.alo[p][2]), "+v"(c.alo[p][3]), "+v"(c.alo[p][4]), "+v"(c.aup[p][0]), "+v"(c.aup[p][1]), "+v"(c.aup[p][2]), "+v"(c.aup[p][3]), "+v"(c.aup[p][4])
#define G_A4(p) "+v"(c.alo[p][0]), "+v"(c.alo[p][1]), "+v"(c.alo[p][2]), "+v"(c.alo[p][3]), "+v"(c.aup[p][0]), "+v"(c.aup[p][1]), "+v"(c.aup[p][2]), "+v"(c.aup[p][3])
#define G_B8(b) "+v"(c.B[b][0][0]), "+v"(c.B[b][0][1]), "+v"(c.B[b][1][0]), "+v"(c.B[b][1][1]), "+v"(c.B[b][2][0]), "+v"(c.B[b][2][1]), "+v"(c.B[b][3][0]), "+v"(c.B[b][3][1])
template <int NTN, int PAR> DI void g_land(GC& c) {
  if constexpr (NTN == 5) asm volatile("s_waitcnt lgkmcnt(0)" : G_A5(PAR) :: "memory"); else asm volatile("s_waitcnt lgkmcnt(0)" : G_A4(PAR) :: "memory");
  GAP();
  #pragma unroll
  for (int ct = 0; ct < NTN; ++ct) c.A[PAR][ct] = __builtin_shufflevector(c.alo[PAR][ct], c.aup[PAR][ct], 0, 1, 2, 3, 4, 5, 6, 7);
  if constexpr (NTN == 5) asm volatile("s_nop 1" : "+v"(c.A[PAR][0]), "+v"(c.A[PAR][1]), "+v"(c.A[PAR][2]), "+v"(c.A[PAR][3]), "+v"(c.A[PAR][4]));
  else                    asm volatile("s_nop 1" : "+v"(c.A[PAR][0]), "+v"(c.A[PAR][1]), "+v"(c.A[PAR][2]), "+v"(c.A[PAR][3]));
  GAP();
}
template <int GCI, int M0, int... Is> DI void g_gaps(GC& c, iseq<Is...>) { (g_gap<GCI, M0 + Is>(c), ...); }
template <int GCI> DI void g_chunk(GC& c) {
  constexpr int nt = g_nt(GCI), n2 = 2 * nt;
  g_gaps<GCI, 0>(c, make_iseq<n2>{});       g_land<nt, 1>(c);
  g_gaps<GCI, n2>(c, make_iseq<n2>{});      g_land<nt, 0>(c);
  g_gaps<GCI, 2 * n2>(c, make_iseq<n2>{});  g_land<nt, 1>(c);
  if constexpr (GCI < 26) { wait_vm<g_wait_bar(GCI)>(); asm volatile("s_barrier" ::: "memory"); GAP(); }
  g_gaps<GCI, 3 * n2>(c, make_iseq<n2>{});
  if constexpr (GCI < 26) {
    asm volatile("s_waitcnt vmcnt(%c8)" : G_B8((GCI + 1) & 1) : "i"(g_wait_end(GCI)) : "memory");
    g_land<g_nt(GCI + 1), 0>(c);
  }
}
template <int... Gs> DI void g_chunks(GC& c, iseq<Gs...>) { (g_chunk<Gs>(c), ...); }
template <int... Ps> DI void g_wdma_all0(GC& c, iseq<Ps...>) { (g_wdma<0, Ps>(c, 0u), ...); }
template <int... Ps> DI void g_wdma_all1(GC& c, iseq<Ps...>) { (g_wdma<1, Ps>(c, 0u), ...); }
template <int... Xs> DI void g_xld_all0(GC& c, iseq<Xs...>) { (g_xld<0, Xs>(c), ...); }
template <int... Xs> DI void g_tr_all0(GC& c, iseq<Xs...>) { (g_trread<0, 0, (Xs >> 1), (Xs & 1)>(c), ...); }
template <int I> DI void q_pack(float sc) {
  constexpr int qb = I >> 5, d0 = (I >> 2) & 7, e = I & 3, src = 16 * (2 * (d0 >> 1) + qb) + 8 * (d0 & 1) + 2 * e;
  const float a = acc_rd<src>(), b = acc_rd<src + 1>();
  acc_wr<128 + 32 * qb + 4 * d0 + e>(cvtpk(a * sc, b * sc));
}
template <int... Is> DI void q_packs(float sc, iseq<Is...>) { (q_pack<Is>(sc), ...); }

DI float w4_aone() { unsigned l; asm volatile("v_mbcnt_lo_u32_b32 %0, -1, 0\n\tv_mbcnt_hi_u32_b32 %0, -1, %0" : "=v"(l)); return l < 32u ? 1.f : 0.f; }
DI void negm_bcast(f32x16& negm, float aone, float neg_mhat) { asm volatile("s_nop 1\n\tv_mfma_f32_32x32x2_f32 %0, %1, %2, 0" : "+v"(negm) : "v"(aone), "v"(neg_mhat)); }
struct W4C {
  unsigned vkb[4], vvb, vksrc[2], vvsrc, m0k, m0v, sofk, sofv;
  srd_t srdK, srdV;
  f32x16 S[2][2][2];
  f32x16 NEGM[2];
  bf16x8 Vf[4][4], Pk[2][4];
  bf16x4 vlo[4][4], vup[4][4];
  float mhat[2], lsum[2], fct[2]; bool pend;
  float rs0[2], rs1[2], pmx0[2], pmx1[2], rmax[2], delta[2];
  unsigned pm0[8], psf[8];
};
constexpr int AO_(int db, int qb) { return 16 * (2 * db + qb); }
constexpr int AQ_(int qb, int d0) { return 128 + 32 * qb + 4 * d0; }
constexpr int AK_(int kb, int d0) { return 192 + 32 * kb + 4 * d0; }
template <int I> DI void tr_read(W4C& c, unsigned vslot) {
  constexpr int ks = I >> 3, db = (I >> 1) & 3;
  if constexpr ((I & 1) == 0) asm volatile("ds_read_b64_tr_b16 %0, %1 offset:%2" : "=v"(c.vlo[db][ks]) : "v"(c.vvb + vslot), "i"(4096 * ks + 512 * db) : "memory");
  else                        asm volatile("ds_read_b64_tr_b16 %0, %1 offset:%2" : "=v"(c.vup[db][ks]) : "v"(c.vvb + vslot), "i"(4096 * ks + 2048 + 512 * db) : "memory");
}
template <int I> DI void k_read(W4C& c, unsigned slot) {
  constexpr int kb = I >> 3, d0 = I & 7;
  asm volatile("ds_read_b128 a[%c1:%c2], %0 offset:%c3" :: "v"(c.vkb[d0 & 3] + slot), "i"(AK_(kb, d0)), "i"(AK_(kb, d0) + 3), "i"(8192 * kb + 128 * (d0 >> 2)) : "memory", ACLOB_HI);
}
template <int X, bool FIRST, int M> DI void qk_mfma(W4C& c) {
  constexpr int d0 = M >> 2, kb = (M >> 1) & 1, qb = M & 1, ak = AK_(kb, d0), aq = AQ_(qb, d0);
  if constexpr (d0 == 0) {
    if constexpr (FIRST) asm volatile("v_mfma_f32_32x32x16_bf16 %0, a[%c1:%c2], a[%c3:%c4], 0" : "=&v"(c.S[X][kb][qb]) : "i"(ak), "i"(ak + 3), "i"(aq), "i"(aq + 3) : ACLOB_LO, ACLOB_HI);
    else                 asm volatile("v_mfma_f32_32x32x16_bf16 %0, a[%c2:%c3], a[%c4:%c5], %1" : "=&v"(c.S[X][kb][qb]) : "v"(c.NEGM[qb]), "i"(ak), "i"(ak + 3), "i"(aq), "i"(aq + 3) : ACLOB_LO, ACLOB_HI);
  } else asm volatile("v_mfma_f32_32x32x16_bf16 %0, a[%c1:%c2], a[%c3:%c4], %0" : "+v"(c.S[X][kb][qb]) : "i"(ak), "i"(ak + 3), "i"(aq), "i"(aq + 3) : ACLOB_LO, ACLOB_HI);
}
template <int M, bool ZERO> DI void pv_mfma(W4C& c) {
  constexpr int ks = M >> 3, db = (M >> 1) & 3, qb = M & 1, ao = AO_(db, qb);
  if constexpr (ZERO && ks == 0) asm volatile("v_mfma_f32_32x32x16_bf16 a[%c2:%c3], %0, %1, 0" :: "v"(c.Vf[db][ks]), "v"(c.Pk[qb][ks]), "i"(ao), "i"(ao + 15) : ACLOB_LO);
  else                           asm volatile("v_mfma_f32_32x32x16_bf16 a[%c2:%c3], %0, %1, a[%c2:%c3]" :: "v"(c.Vf[db][ks]), "v"(c.Pk[qb][ks]), "i"(ao), "i"(ao + 15) : ACLOB_LO);
}
template <int X, int QB, int KS, int P> DI void pack1(W4C& c) {
  u32x4 w = __builtin_bit_cast(u32x4, c.Pk[QB][KS]);
  w[P] = cvtpk(c.S[X][KS >> 1][QB][8 * (KS & 1) + 2 * P], c.S[X][KS >> 1][QB][8 * (KS & 1) + 2 * P + 1]);
  c.Pk[QB][KS] = __builtin_bit_cast(bf16x8, w);
}
template <int X, int J> DI void rowsum1(W4C& c) {
  constexpr int qb = J >> 4, ks = (J >> 2) & 3, p = J & 3;
  c.rs0[qb] = fadd_s(c.rs0[qb], c.S[X][ks >> 1][qb][8 * (ks & 1) + 2 * p]); c.rs1[qb] = fadd_s(c.rs1[qb], c.S[X][ks >> 1][qb][8 * (ks & 1) + 2 * p + 1]);
}
template <int X, bool FIRST, int M> DI void gapA(W4C& c, unsigned vslot, const int t) {
  constexpr int Xp = X ^ 1;
  qk_mfma<X, FIRST, M>(c);
  if constexpr (M >= 16 && M <= 31) {
    constexpr int q = (M - 16) >> 1;
    if constexpr (((M - 16) & 1) == 0) {
      if constexpr (q < 4) c.pm0[q] = c.m0k + (unsigned)((t + 2) & 1) * 0x4000u + 1024u * q; else c.pm0[q] = c.m0v + (unsigned)((t + 1) & 1) * 0x4000u + 1024u * (q - 4);
      asm volatile("" : "+s"(c.pm0[q]));
    } else {
      if constexpr (q < 4) c.psf[q] = KTILEB * (unsigned)(t + 2) + c.sofk + KRS * 4u * q; else c.psf[q] = KTILEB * (unsigned)(t + 1) + c.sofv + KRS * 8u * ((q - 4) >> 1) + 128u * ((q - 4) & 1);
      asm volatile("" : "+s"(c.psf[q]));
    }
  }
  if constexpr (!FIRST && M == 8) asm volatile("" :: "v"(c.NEGM[0]), "v"(c.NEGM[1]));
  if constexpr (!FIRST) {
    constexpr int base = M + (M >= 4) + (M >= 8) + (M >= 12) + (M >= 16) + (M >= 20) + (M >= 24);
    if constexpr (M <= 25) tr_read<base>(c, vslot);
    if constexpr (M == 3 || M == 7 || M == 11 || M == 15 || M == 19 || M == 23) tr_read<base + 1>(c, vslot);
    if constexpr (M < 16) {
      constexpr int e0 = 2 * M, e1 = 2 * M + 1;
      c.S[Xp][1][e0 >> 4][e0 & 15] = __builtin_amdgcn_exp2f(c.S[Xp][1][e0 >> 4][e0 & 15]);
      c.S[Xp][1][e1 >> 4][e1 & 15] = __builtin_amdgcn_exp2f(c.S[Xp][1][e1 >> 4][e1 & 15]);
      pack1<Xp, (M >> 3), ((M >> 2) & 1), (M & 3)>(c);
    } else {
      constexpr int cc = M - 16;
      pack1<Xp, (cc >> 3), 2 + ((cc >> 2) & 1), (cc & 3)>(c);
      rowsum1<Xp, 16 * (cc >> 3) + 8 + 4 * ((cc >> 2) & 1) + (cc & 3)>(c);
    }
  }
  GAP();
}
#define W4_VLO "+v"(c.vlo[0][0]), "+v"(c.vlo[0][1]), "+v"(c.vlo[0][2]), "+v"(c.vlo[0][3]), "+v"(c.vlo[1][0]), "+v"(c.vlo[1][1]), "+v"(c.vlo[1][2]), "+v"(c.vlo[1][3]), \
               "+v"(c.vlo[2][0]), "+v"(c.vlo[2][1]), "+v"(c.vlo[2][2]), "+v"(c.vlo[2][3]), "+v"(c.vlo[3][0]), "+v"(c.vlo[3][1]), "+v"(c.vlo[3][2]), "+v"(c.vlo[3][3])
#define W4_VUP "+v"(c.vup[0][0]), "+v"(c.vup[0][1]), "+v"(c.vup[0][2]), "+v"(c.vup[0][3]), "+v"(c.vup[1][0]), "+v"(c.vup[1][1]), "+v"(c.vup[1][2]), "+v"(c.vup[1][3]), \
               "+v"(c.vup[2][0]), "+v"(c.vup[2][1]), "+v"(c.vup[2][2]), "+v"(c.vup[2][3]), "+v"(c.vup[3][0]), "+v"(c.vup[3][1]), "+v"(c.vup[3][2]), "+v"(c.vup[3][3])
template <int X, bool FIRST, int... Ms> DI void phaseA_(W4C& c, const int t, iseq<Ms...>) {
  const unsigned vslot = (unsigned)((t - 1) & 1) * 0x4000u;
  (gapA<X, FIRST, Ms>(c, vslot, t), ...);
  if constexpr (!FIRST) {
    #pragma unroll
    for (int qb = 0; qb < 2; ++qb) {
      const float rs = fadd_s(c.rs0[qb], c.rs1[qb]);
      auto pr = __builtin_amdgcn_permlane32_swap(__float_as_uint(rs), __float_as_uint(rs), false, false);
      c.lsum[qb] = fadd_s(c.lsum[qb], fadd_s(__uint_as_float(pr[0]), __uint_as_float(pr[1])));
    }
  }
  if constexpr (FIRST) asm volatile("s_waitcnt vmcnt(0) lgkmcnt(0)\n\ts_barrier" ::: "memory");
  else { asm volatile("s_waitcnt vmcnt(0) lgkmcnt(0)" : W4_VLO :: "memory"); asm volatile("s_barrier" : W4_VUP :: "memory"); }
  GAP();
  if constexpr (!FIRST) {
    #pragma unroll
    for (int ks = 0; ks < 4; ++ks)
      #pragma unroll
      for (int db = 0; db < 4; ++db) c.Vf[db][ks] = __builtin_shufflevector(c.vlo[db][ks], c.vup[db][ks], 0, 1, 2, 3, 4, 5, 6, 7);
  }
}
template <int X, bool FIRST> DI void phaseA(W4C& c, const int t) { phaseA_<X, FIRST>(c, t, make_iseq<32>{}); }
template <int R> DI void o_scale1(float f0, float f1) {
  float t; asm volatile("v_accvgpr_read_b32 %0, a[%c2]\n\tv_mul_f32 %0, %1, %0\n\tv_accvgpr_write_b32 a[%c2], %0" : "=&v"(t) : "v"(((R >> 4) & 1) ? f1 : f0), "i"(R) : ACLOB_LO);
}
template <int... Rs> DI void o_scale(float f0, float f1, iseq<Rs...>) { (o_scale1<Rs>(f0, f1), ...); }
template <int X, bool PEEL, bool ZERO1, bool DK, bool DV, bool KR, int M> DI void gapB(W4C& c, const unsigned kslot_rd) {
  if constexpr (!PEEL) pv_mfma<M, ZERO1>(c);
  if constexpr (M < 4 && DK)                dma16(c.pm0[M], c.vksrc[M & 1], c.srdK, c.psf[M]);
  else if constexpr (M >= 4 && M < 8 && DV) dma16(c.pm0[M], c.vvsrc, c.srdV, c.psf[M]);
  if constexpr (M >= 1 && M <= 16 && KR) k_read<M - 1>(c, kslot_rd);
  if constexpr (M >= 8 && M < 16) {
    constexpr int i = M - 8, q = i >> 2, r = 4 * (i & 3);
    if constexpr ((i & 3) == 0) { c.pmx0[q] = max3f(c.S[X][0][q][0], c.S[X][0][q][1], c.S[X][1][q][0]); c.pmx1[q] = max3f(c.S[X][0][q][2], c.S[X][0][q][3], c.S[X][1][q][1]);
                                  c.pmx0[q] = max3f(c.pmx0[q], c.S[X][1][q][2], c.S[X][1][q][3]); }
    else                        { c.pmx0[q] = max3f(c.pmx0[q], c.S[X][0][q][r], c.S[X][0][q][r + 1]); c.pmx1[q] = max3f(c.pmx1[q], c.S[X][0][q][r + 2], c.S[X][0][q][r + 3]);
                                  c.pmx0[q] = max3f(c.pmx0[q], c.S[X][1][q][r], c.S[X][1][q][r + 1]); c.pmx1[q] = max3f(c.pmx1[q], c.S[X][1][q][r + 2], c.S[X][1][q][r + 3]); }
  }
  if constexpr (M == 16 || M == 17) {
    constexpr int q = M - 16; const float mm = max2f(c.pmx0[q], c.pmx1[q]);
    auto pr = __builtin_amdgcn_permlane32_swap(__float_as_uint(mm), __float_as_uint(mm), false, false);
    c.rmax[q] = max2f(__uint_as_float(pr[0]), __uint_as_float(pr[1]));
  }
  if constexpr (M == 18) {
    if constexpr (PEEL) {
      #pragma unroll
      for (int q = 0; q < 2; ++q) { c.mhat[q] = c.rmax[q]; c.lsum[q] = 0.f; c.fct[q] = 1.f;
        #pragma unroll
        for (int kb = 0; kb < 2; ++kb) { _Pragma("unroll") for (int e = 0; e < 16; ++e) c.S[X][kb][q][e] = fsub_s(c.S[X][kb][q][e], c.rmax[q]); }
        negm_bcast(c.NEGM[q], w4_aone(), -c.mhat[q]);
      }
    } else {
      const bool grew = (c.rmax[0] > THR) | (c.rmax[1] > THR);
      if (__builtin_expect(__any((int)grew), 0)) {
        #pragma unroll
        for (int q = 0; q < 2; ++q) { const float dl = max2f(c.rmax[q], 0.f); c.delta[q] = dl; c.mhat[q] = fadd_s(c.mhat[q], dl); c.fct[q] = __builtin_amdgcn_exp2f(-dl);
          #pragma unroll
          for (int kb = 0; kb < 2; ++kb) { _Pragma("unroll") for (int e = 0; e < 16; ++e) c.S[X][kb][q][e] = fsub_s(c.S[X][kb][q][e], dl); }
          negm_bcast(c.NEGM[q], w4_aone(), -c.mhat[q]);
        }
        c.pend = true;
      }
    }
  }
  if constexpr (M == 18) { c.rs0[0] = c.rs0[1] = c.rs1[0] = c.rs1[1] = 0.f; }
  if constexpr (M >= 19) {
    constexpr int g = M - 19, e_lo = (32 * g) / 13, e_hi = (32 * (g + 1)) / 13;
    {
      constexpr int p_lo = g ? (32 * (g - 1)) / 13 : 0, p_hi = g ? (32 * g) / 13 : 0;
      if constexpr (p_lo + 0 < p_hi) { constexpr int e = p_lo + 0; if constexpr (e & 1) c.rs1[e >> 4] = fadd_s(c.rs1[e >> 4], c.S[X][0][e >> 4][e & 15]); else c.rs0[e >> 4] = fadd_s(c.rs0[e >> 4], c.S[X][0][e >> 4][e & 15]); }
      if constexpr (p_lo + 1 < p_hi) { constexpr int e = p_lo + 1; if constexpr (e & 1) c.rs1[e >> 4] = fadd_s(c.rs1[e >> 4], c.S[X][0][e >> 4][e & 15]); else c.rs0[e >> 4] = fadd_s(c.rs0[e >> 4], c.S[X][0][e >> 4][e & 15]); }
      if constexpr (p_lo + 2 < p_hi) { constexpr int e = p_lo + 2; if constexpr (e & 1) c.rs1[e >> 4] = fadd_s(c.rs1[e >> 4], c.S[X][0][e >> 4][e & 15]); else c.rs0[e >> 4] = fadd_s(c.rs0[e >> 4], c.S[X][0][e >> 4][e & 15]); }
    }
    if constexpr (e_lo + 0 < e_hi) c.S[X][0][(e_lo + 0) >> 4][(e_lo + 0) & 15] = __builtin_amdgcn_exp2f(c.S[X][0][(e_lo + 0) >> 4][(e_lo + 0) & 15]);
    if constexpr (e_lo + 1 < e_hi) c.S[X][0][(e_lo + 1) >> 4][(e_lo + 1) & 15] = __builtin_amdgcn_exp2f(c.S[X][0][(e_lo + 1) >> 4][(e_lo + 1) & 15]);
    if constexpr (e_lo + 2 < e_hi) c.S[X][0][(e_lo + 2) >> 4][(e_lo + 2) & 15] = __builtin_amdgcn_exp2f(c.S[X][0][(e_lo + 2) >> 4][(e_lo + 2) & 15]);
  }
  if constexpr (M == 31) asm volatile("" :: "v"(c.S[X][0][0]), "v"(c.S[X][0][1]), "v"(c.NEGM[0]), "v"(c.NEGM[1]), "v"(c.rs0[0]), "v"(c.rs0[1]), "v"(c.rs1[0]), "v"(c.rs1[1]));
  GAP();
}
template <int X, bool PEEL, bool ZERO1, bool DK, bool DV, bool KR, int... Ms> DI void phaseB_(W4C& c, const int t, iseq<Ms...>) {
  const unsigned kslot_rd = (unsigned)((t + 1) & 1) * 0x4000u;
  if constexpr (!PEEL) asm volatile("s_nop 1" ::: "memory");
  GAP();
  (gapB<X, PEEL, ZERO1, DK, DV, KR, Ms>(c, kslot_rd), ...);
  {
    #pragma unroll
    for (int e = (32 * 12) / 13; e < 32; ++e) { if (e & 1) c.rs1[e >> 4] = fadd_s(c.rs1[e >> 4], c.S[X][0][e >> 4][e & 15]); else c.rs0[e >> 4] = fadd_s(c.rs0[e >> 4], c.S[X][0][e >> 4][e & 15]); }
  }
  if constexpr (!PEEL) {
    if (__builtin_expect(c.pend, 0)) {
      asm volatile("s_nop 7\n\ts_nop 7" ::: "memory");
      o_scale(c.fct[0], c.fct[1], make_iseq<128>{});
      asm volatile("s_nop 1" ::: "memory");
      c.lsum[0] = fmul_s(c.lsum[0], c.fct[0]); c.lsum[1] = fmul_s(c.lsum[1], c.fct[1]); c.pend = false;
    }
  }
  asm volatile("s_waitcnt lgkmcnt(0)" ::: "memory");
  GAP();
}
template <int X, bool PEEL, bool ZERO1, bool DK = true, bool DV = true, bool KR = true> DI void phaseB(W4C& c, const int t) { phaseB_<X, PEEL, ZERO1, DK, DV, KR>(c, t, make_iseq<32>{}); }
template <int... Is> DI void k_reads(W4C& c, unsigned slot, iseq<Is...>) { (k_read<Is>(c, slot), ...); }
template <int... Is> DI void tr_reads(W4C& c, unsigned vslot, iseq<Is...>) { (tr_read<Is>(c, vslot), ...); }
template <int X, int... Cs> DI void packs(W4C& c, iseq<Cs...>) { (pack1<X, ((Cs >> 3) & 1), 2 * (Cs >> 4) + ((Cs >> 2) & 1), (Cs & 3)>(c), ...); }
template <int X, int J> DI void rowsum1_padded(W4C& c) { rowsum1<X, J>(c); asm volatile("s_nop 1"); }
template <int X, int... Js> DI void rowsums(W4C& c, iseq<Js...>) { (rowsum1_padded<X, Js>(c), ...); }
template <int... Ms> DI void pv_all(W4C& c, iseq<Ms...>) { (pv_mfma<Ms, false>(c), ...); }
template <int F> DI void o_store(float il, srd_t srdO, unsigned voff, unsigned soff) {
  constexpr int qb = F >> 3, st = F & 7, src = 16 * (2 * (st >> 1) + qb) + 8 * (st & 1);
  u32x4 w;
  w[0] = cvtpk(acc_rd<src + 0>() * il, acc_rd<src + 1>() * il); w[1] = cvtpk(acc_rd<src + 2>() * il, acc_rd<src + 3>() * il);
  w[2] = cvtpk(acc_rd<src + 4>() * il, acc_rd<src + 5>() * il); w[3] = cvtpk(acc_rd<src + 6>() * il, acc_rd<src + 7>() * il);
  __builtin_amdgcn_raw_buffer_store_b128(w, srdO, voff, soff + (unsigned)(qb * 256 + st) * 1024u, 0);
}
template <int... Fs> DI void o_stores(float il0, float il1, srd_t srdO, unsigned voff, unsigned soff, iseq<Fs...>) { (o_store<Fs>((Fs >> 3) ? il1 : il0, srdO, voff, soff), ...); }

__global__ __launch_bounds__(256, 1)
void k2_main(const unsigned short* __restrict__ Kc, const unsigned short* __restrict__ Vc, const unsigned short* __restrict__ Wq,
             const unsigned short* __restrict__ Wg, const unsigned short* __restrict__ Wu,
             const unsigned short* __restrict__ xn, unsigned short* __restrict__ hbuf, unsigned short* __restrict__ obuf) {
  extern __shared__ __attribute__((aligned(16))) char lds[];
  const unsigned tid = threadIdx.x, lane = tid & 63u, l32 = lane & 31u, hi = lane >> 5;
  const unsigned wid = rfl(tid >> 6);
  const unsigned ldsb = (unsigned)(size_t)lds;
  const unsigned bx = blockIdx.x, idx = bx >> 3, j = (bx & 7u) * 4u + (idx & 3u), rb = idx >> 2;
  const unsigned tb0 = rb * 8u + 2u * wid;
  float lsum0, lsum1;
  {
    GC g;
    const unsigned q4 = (lane >> 2) & 3u, p4 = lane & 3u, blk = (lane >> 4) & 1u, isU = wid >> 1, wl = wid & 1u;
    const unsigned rowu = (4u * hi + q4) ^ (4u * blk);
    g.trb[0] = ldsb + WRING + rowu * 32u + p4 * 8u + blk * 8192u;
    g.trb[1] = ldsb + WRING + rowu * 32u + p4 * 8u + blk * 10240u;
    g.trb[2] = ldsb + WRING + (blk << 5) + ((p4 & 1u) << 4) + ((p4 >> 1) << 3) + (q4 << 6) + (hi << 8);
    const unsigned rowx = ((lane >> 1) & 7u) ^ (4u * isU);
    g.wsrc1 = rowx * (FF * 2u) + (lane >> 4) * 32u + (lane & 1u) * 16u;
    #pragma unroll
    for (int p = 0; p < 5; ++p) { const unsigned u = 4u * p + (lane >> 4), rg = u / 5u, ct = u - 5u * rg; g.wsrc2[p] = (8u * rg + rowx) * (FF * 2u) + ct * 32u + (lane & 1u) * 16u; }
    g.wsrc3 = 8192u * ((lane >> 2) & 7u) + 64u * hi + 16u * (lane & 3u);
    g.xvo[0] = lane * 16u; g.xvo[1] = lane * 16u + 36864u; g.hvo = lane * 16u;
    g.srdX = mk_srd((const char*)xn + (size_t)tb0 * 36864u, 2u * 36864u);
    g.srdGU = mk_srd(isU ? Wu : Wg, DM * FF * 2u);
    g.srdWq = mk_srd(Wq, DM * NH * DH * 2u);
    g.srdH = mk_srd((char*)hbuf + (size_t)tb0 * 288u * 1024u, 2u * 288u * 1024u);
    g.srdK = mk_srd((const char*)Kc + (size_t)j * SEQ * 256u, SEQ * 256u);
    g.srdV = mk_srd((const char*)Vc + (size_t)j * SEQ * 256u, SEQ * 256u);
    g.m0g1 = ldsb + WRING + isU * 8192u + wl * 4096u;   g.sof1 = wl * 32u * (FF * 2u) + j * 288u;
    g.m0g2 = ldsb + WRING + isU * 10240u + wl * 5120u;  g.sof2 = wl * 32u * (FF * 2u) + j * 288u + 128u;
    g.m0q = ldsb + WRING + wid * 4096u;                 g.sofq = wid * 16u * 8192u + j * 256u;
    g.hso = j * 9u * 1024u;
    #pragma unroll
    for (int par = 0; par < 2; ++par) g.vksrc[par] = KRS * (lane >> 4) + 16u * ((lane & 15u) ^ (4u * par + (lane >> 4)));
    g.vvsrc = KRS * ((lane >> 2) & 7u) + 64u * hi + 16u * (lane & 3u);
    g.m0k = ldsb + KRING + 4096u * wid; g.m0v = ldsb + VRING + 4096u * wid; g.sofk = KRS * 16u * wid; g.sofv = KRS * 16u * wid;
    g.kc0 = -2.302208198f; g.kc1 = -0.1029432397f;
    asm volatile("" : "+v"(g.kc0), "+v"(g.kc1));
    asm volatile("s_nop 4" ::: "memory");
    g_wdma_all0(g, make_iseq<4>{}); g_xld_all0(g, make_iseq<8>{}); g_wdma_all1(g, make_iseq<4>{});
    { GC& c = g; asm volatile("s_waitcnt vmcnt(4)\n\ts_barrier" : G_B8(0) :: "memory"); }
    g_tr_all0(g, make_iseq<8>{});
    g_land<4, 0>(g);
    g_chunks(g, make_iseq<27>{});
    asm volatile("s_nop 7\n\ts_nop 7" ::: "memory");
    q_packs(0.12751743082f, make_iseq<64>{});
  }
  W4C c;
  #pragma unroll
  for (int jj = 0; jj < 4; ++jj) c.vkb[jj] = ldsb + KRING + 256u * l32 + 16u * ((2u * jj + hi) ^ (l32 & 7u));
  c.vvb = ldsb + VRING + (((lane & 3u) << 3) | (((lane >> 2) & 3u) << 6) | (((lane >> 4) & 1u) << 5) | (hi << 8));
  #pragma unroll
  for (int par = 0; par < 2; ++par) c.vksrc[par] = KRS * (lane >> 4) + 16u * ((lane & 15u) ^ (4u * par + (lane >> 4)));
  c.vvsrc = KRS * ((lane >> 2) & 7u) + 64u * hi + 16u * (lane & 3u);
  c.m0k = ldsb + KRING + 4096u * wid; c.m0v = ldsb + VRING + 4096u * wid; c.sofk = KRS * 16u * wid; c.sofv = KRS * 16u * wid;
  c.srdK = mk_srd((const char*)Kc + (size_t)j * SEQ * 256u, SEQ * 256u);
  c.srdV = mk_srd((const char*)Vc + (size_t)j * SEQ * 256u, SEQ * 256u);
  asm volatile("s_waitcnt vmcnt(0)\n\ts_barrier" ::: "memory");
  k_reads(c, 0u, make_iseq<16>{});
  asm volatile("s_waitcnt lgkmcnt(0)\n\ts_nop 1" ::: "memory");
  c.mhat[0] = c.mhat[1] = 0.f; c.lsum[0] = c.lsum[1] = 0.f; c.fct[0] = c.fct[1] = 1.f; c.pend = false;
  constexpr int T = NT_KV;
  phaseA<0, true>(c, 0);  phaseB<0, true,  false>(c, 0);
  phaseA<1, false>(c, 1); phaseB<1, false, true>(c, 1);
  for (int t = 2; t < T - 2; t += 2) {
    phaseA<0, false>(c, t);     phaseB<0, false, false>(c, t);
    phaseA<1, false>(c, t + 1); phaseB<1, false, false>(c, t + 1);
  }
  phaseA<0, false>(c, T - 2); phaseB<0, false, false, false, true, true>(c, T - 2);
  phaseA<1, false>(c, T - 1); phaseB<1, false, false, false, false, false>(c, T - 1);
  {
    const unsigned vslot = (unsigned)((T - 1) & 1) * 0x4000u;
    tr_reads(c, vslot, make_iseq<32>{});
    #pragma unroll
    for (int e = 0; e < 32; ++e) c.S[1][1][e >> 4][e & 15] = __builtin_amdgcn_exp2f(c.S[1][1][e >> 4][e & 15]);
    packs<1>(c, make_iseq<32>{});
    rowsums<1>(c, iseq<8, 9, 10, 11, 12, 13, 14, 15, 24, 25, 26, 27, 28, 29, 30, 31>{});
    #pragma unroll
    for (int qb = 0; qb < 2; ++qb) { const float rs = fadd_s(c.rs0[qb], c.rs1[qb]);
      auto pr = __builtin_amdgcn_permlane32_swap(__float_as_uint(rs), __float_as_uint(rs), false, false);
      c.lsum[qb] = fadd_s(c.lsum[qb], fadd_s(__uint_as_float(pr[0]), __uint_as_float(pr[1]))); }
    asm volatile("s_waitcnt lgkmcnt(0)" : W4_VLO :: "memory"); asm volatile("" : W4_VUP :: "memory");
    GAP();
    #pragma unroll
    for (int ks = 0; ks < 4; ++ks)
      #pragma unroll
      for (int db = 0; db < 4; ++db) c.Vf[db][ks] = __builtin_shufflevector(c.vlo[db][ks], c.vup[db][ks], 0, 1, 2, 3, 4, 5, 6, 7);
    asm volatile("s_nop 1" ::: "memory");
    pv_all(c, make_iseq<32>{});
    asm volatile("s_nop 7\n\ts_nop 7" ::: "memory");
  }
  lsum0 = c.lsum[0]; lsum1 = c.lsum[1];
  {
    const srd_t srdO = mk_srd((char*)obuf + (size_t)tb0 * 256u * 1024u, 2u * 256u * 1024u);
    o_stores(__builtin_amdgcn_rcpf(lsum0), __builtin_amdgcn_rcpf(lsum1), srdO, lane * 16u, j * 8u * 1024u, make_iseq<16>{});
  }
}

constexpr unsigned K3SLOT = 36864u;
constexpr size_t SHM_K3 = 3 * 36864;
typedef __attribute__((address_space(1))) unsigned gu32;
struct K3C {
  unsigned trb, wsrc[9], bvo[2];
  srd_t srdW, srdA;
  unsigned m0w, sofw;
  bf16x8 B[2][4][2];
  bf16x4 alo[2][9], aup[2][9];
  bf16x8 A[2][9];
  f32x4 acc[9][2];
};
template <int ST, int CT, int HALF> DI void k3_tr(K3C& c, unsigned base) {
  constexpr int off = ((4 * ST + HALF) * 9 + CT) * 256, par = ST & 1;
  if constexpr (HALF == 0) asm volatile("ds_read_b64_tr_b16 %0, %1 offset:%c2" : "=v"(c.alo[par][CT]) : "v"(base), "i"(off) : "memory");
  else                     asm volatile("ds_read_b64_tr_b16 %0, %1 offset:%c2" : "=v"(c.aup[par][CT]) : "v"(base), "i"(off) : "memory");
}
template <int BUF, int X> DI void k3_bld(K3C& c, unsigned soff) {
  constexpr int st = X >> 1, tg = X & 1;
  asm volatile("buffer_load_dwordx4 %0, %1, %2, %3 offen offset:%c4" : "=v"(c.B[BUF][st][tg]) : "v"(c.bvo[tg]), "s"(c.srdA), "s"(soff + (st >> 1) * 4096u), "i"((st & 1) * 2048) : "memory");
}
template <int BUF, int ST, int M> DI void k3_gap(K3C& c, unsigned trcur, unsigned trnext, unsigned bsoff, unsigned wm0, unsigned wsoff) {
  constexpr int ct = M >> 1, tg = M & 1;
  asm volatile("v_mfma_f32_16x16x32_bf16 %0, %1, %2, %0" : "+v"(c.acc[ct][tg]) : "v"(c.A[ST & 1][ct]), "v"(c.B[BUF][ST][tg]));
  if constexpr (M < 9) { constexpr int ns = (ST + 1) & 3; k3_tr<ns, M, 0>(c, ST == 3 ? trnext : trcur); k3_tr<ns, M, 1>(c, ST == 3 ? trnext : trcur); }
  if constexpr (ST == 0 && M >= 9 && M < 17) k3_bld<BUF ^ 1, M - 9>(c, bsoff);
  if constexpr (ST == 1 && M >= 9 && (M & 1) == 1) dma16(wm0 + 1024u * ((M - 9) >> 1), c.wsrc[(M - 9) >> 1], c.srdW, wsoff);
  if constexpr (ST == 2 && M >= 10 && (M & 1) == 0) dma16(wm0 + 1024u * (5 + ((M - 10) >> 1)), c.wsrc[5 + ((M - 10) >> 1)], c.srdW, wsoff);
  GAP();
}
#define K3_A(p) "+v"(c.alo[p][0]), "+v"(c.alo[p][1]), "+v"(c.alo[p][2]), "+v"(c.alo[p][3]), "+v"(c.alo[p][4]), "+v"(c.alo[p][5]), "+v"(c.alo[p][6]), "+v"(c.alo[p][7]), "+v"(c.alo[p][8]), \
                "+v"(c.aup[p][0]), "+v"(c.aup[p][1]), "+v"(c.aup[p][2]), "+v"(c.aup[p][3]), "+v"(c.aup[p][4]), "+v"(c.aup[p][5]), "+v"(c.aup[p][6]), "+v"(c.aup[p][7]), "+v"(c.aup[p][8])
template <int PAR> DI void k3_land(K3C& c) {
  asm volatile("s_waitcnt lgkmcnt(0)" : K3_A(PAR) :: "memory");
  GAP();
  #pragma unroll
  for (int ct = 0; ct < 9; ++ct) c.A[PAR][ct] = __builtin_shufflevector(c.alo[PAR][ct], c.aup[PAR][ct], 0, 1, 2, 3, 4, 5, 6, 7);
  asm volatile("s_nop 1" : "+v"(c.A[PAR][0]), "+v"(c.A[PAR][1]), "+v"(c.A[PAR][2]), "+v"(c.A[PAR][3]), "+v"(c.A[PAR][4]), "+v"(c.A[PAR][5]), "+v"(c.A[PAR][6]), "+v"(c.A[PAR][7]), "+v"(c.A[PAR][8]));
  GAP();
}
template <int BUF, int ST, int... Ms> DI void k3_step(K3C& c, unsigned trcur, unsigned trnext, unsigned bsoff, unsigned wm0, unsigned wsoff, iseq<Ms...>) { (k3_gap<BUF, ST, Ms>(c, trcur, trnext, bsoff, wm0, wsoff), ...); }
template <int BUF> DI void k3_chunk(K3C& c, unsigned trcur, unsigned trnext, unsigned bsoff, unsigned wm0, unsigned wsoff) {
  k3_step<BUF, 0>(c, trcur, trnext, bsoff, wm0, wsoff, make_iseq<18>{}); k3_land<1>(c);
  k3_step<BUF, 1>(c, trcur, trnext, bsoff, wm0, wsoff, make_iseq<18>{}); k3_land<0>(c);
  k3_step<BUF, 2>(c, trcur, trnext, bsoff, wm0, wsoff, make_iseq<18>{}); k3_land<1>(c);
  asm volatile("s_waitcnt vmcnt(17)\n\ts_barrier" ::: "memory"); GAP();
  k3_step<BUF, 3>(c, trcur, trnext, bsoff, wm0, wsoff, make_iseq<18>{});
  asm volatile("s_waitcnt vmcnt(9)" : G_B8(BUF ^ 1) :: "memory");
  k3_land<0>(c);
}
template <int... Xs> DI void k3_bld_all(K3C& c, unsigned soff, iseq<Xs...>) { (k3_bld<0, Xs>(c, soff), ...); }
template <int... Xs> DI void k3_tr_all(K3C& c, unsigned base, iseq<Xs...>) { (k3_tr<0, (Xs >> 1), (Xs & 1)>(c, base), ...); }

__global__ __launch_bounds__(256, 1)
void k3_out(const unsigned short* __restrict__ x, const unsigned short* __restrict__ Wo, const unsigned short* __restrict__ Wd,
            const unsigned short* __restrict__ obuf, const unsigned short* __restrict__ hbuf,
            float* __restrict__ slab, unsigned* __restrict__ flags, unsigned short* __restrict__ out) {
  extern __shared__ __attribute__((aligned(16))) char lds[];
  const unsigned tid = threadIdx.x, lane = tid & 63u;
  const unsigned wid = rfl(tid >> 6);
  const unsigned ldsb = (unsigned)(size_t)lds;
  const unsigned bx = blockIdx.x, xcd = bx & 7u, idx = bx >> 3, role = xcd >> 1, mt = (xcd & 1u) * 8u + (idx >> 2), nt = idx & 3u;
  const bool isF = role >= 2u;
  const unsigned nchunk = isF ? 18u : 16u, steps_row = isF ? 288u : 256u;
  const unsigned k0 = (role & 1u) * nchunk * 128u;
  const unsigned tb = mt * 4u + wid;
  K3C c;
  const unsigned kg = lane >> 4, q4 = (lane >> 2) & 3u, p4 = lane & 3u;
  c.trb = ldsb + (kg >> 1) * (2u * 9u * 256u) + (4u * (kg & 1u) + q4) * 32u + p4 * 8u;
  #pragma unroll
  for (int p = 0; p < 9; ++p) { const unsigned u = 4u * p + kg, rg = u / 9u, ct = u - 9u * rg; c.wsrc[p] = (8u * rg + ((lane >> 1) & 7u)) * (DM * 2u) + ct * 32u + (lane & 1u) * 16u; }
  #pragma unroll
  for (int tg = 0; tg < 2; ++tg) c.bvo[tg] = (kg >> 1) * 1024u + (kg & 1u) * 512u + (16u * tg + (lane & 15u)) * 16u;
  c.srdW = mk_srd(isF ? Wd : Wo, (isF ? FF : NH * DH) * DM * 2u);
  c.srdA = mk_srd((const char*)(isF ? hbuf : obuf) + (size_t)tb * steps_row * 1024u, steps_row * 1024u);
  c.m0w = ldsb + wid * 9216u;
  c.sofw = (k0 + wid * 32u) * (DM * 2u) + nt * 288u;
  const unsigned bs0 = (k0 >> 4) * 1024u;
  #pragma unroll
  for (int ct = 0; ct < 9; ++ct) { c.acc[ct][0] = (f32x4){0.f, 0.f, 0.f, 0.f}; c.acc[ct][1] = (f32x4){0.f, 0.f, 0.f, 0.f}; }
  asm volatile("s_nop 4" ::: "memory");
  #pragma unroll
  for (int p = 0; p < 9; ++p) dma16(c.m0w + 1024u * p, c.wsrc[p], c.srdW, c.sofw);
  k3_bld_all(c, bs0, make_iseq<8>{});
  #pragma unroll
  for (int p = 0; p < 9; ++p) dma16(c.m0w + K3SLOT + 1024u * p, c.wsrc[p], c.srdW, c.sofw + 128u * (DM * 2u));
  asm volatile("s_waitcnt vmcnt(9)\n\ts_barrier" : G_B8(0) :: "memory");
  k3_tr_all(c, c.trb, make_iseq<18>{});
  k3_land<0>(c);
  unsigned slot = 0;
  const unsigned last = nchunk - 1u;
  for (unsigned ch = 0; ch < nchunk; ch += 2u) {
    {
      const unsigned s1 = slot == 2u ? 0u : slot + 1u, s2 = s1 == 2u ? 0u : s1 + 1u, c1 = ch + 1u, c2 = (ch + 2u > last) ? last : ch + 2u;
      k3_chunk<0>(c, c.trb + slot * K3SLOT, c.trb + s1 * K3SLOT, bs0 + c1 * 8192u, c.m0w + s2 * K3SLOT, c.sofw + c2 * 128u * (DM * 2u));
      slot = s1;
    }
    {
      const unsigned s1 = slot == 2u ? 0u : slot + 1u, s2 = s1 == 2u ? 0u : s1 + 1u, c1 = (ch + 2u > last) ? last : ch + 2u, c2 = (ch + 3u > last) ? last : ch + 3u;
      k3_chunk<1>(c, c.trb + slot * K3SLOT, c.trb + s1 * K3SLOT, bs0 + c1 * 8192u, c.m0w + s2 * K3SLOT, c.sofw + c2 * 128u * (DM * 2u));
      slot = s1;
    }
  }
  asm volatile("s_waitcnt vmcnt(0) lgkmcnt(0)\n\ts_nop 7\n\ts_nop 7" : G_B8(0) :: "memory");
  const unsigned tile = mt * 4u + nt;
  const srd_t srdS = mk_srd(slab, 256u * 4u * 18u * 1024u);
  const unsigned mybase = ((tile * 4u + role) * 4u + wid) * 18u * 1024u;
  #pragma unroll
  for (int i = 0; i < 18; ++i)
    if ((unsigned)(i & 3) != role) __builtin_amdgcn_raw_buffer_store_b128(__builtin_bit_cast(u32x4, c.acc[i >> 1][i & 1]), srdS, lane * 16u, mybase + i * 1024u, 16);
  asm volatile("s_waitcnt vmcnt(0)" ::: "memory");
  __syncthreads();
  gu32* fl = (gu32*)flags;
  if (tid == 0) __hip_atomic_store(fl + tile * 4u + role, 1u, __ATOMIC_RELAXED, __HIP_MEMORY_SCOPE_AGENT);
  if (wid == 0) {
    #pragma unroll
    for (unsigned r = 1; r < 4; ++r) {
      gu32* w = fl + tile * 4u + ((role + r) & 3u);
      while ((unsigned)__builtin_amdgcn_readfirstlane(__hip_atomic_load(w, __ATOMIC_RELAXED, __HIP_MEMORY_SCOPE_AGENT)) != 1u) __builtin_amdgcn_s_sleep(1);
    }
    __builtin_amdgcn_fence(__ATOMIC_ACQUIRE, "agent");
    asm volatile("s_waitcnt vmcnt(0)" ::: "memory");
  }
  __syncthreads();
  f32x4 part[5][4]; u32x2 xr[5];
  const unsigned tok0 = mt * 128u + wid * 32u + (lane & 15u), col0 = nt * 144u + 4u * (lane >> 4);
  #pragma unroll
  for (int n = 0; n < 5; ++n) {
    const unsigned i = role + 4u * n;
    if (i < 18u) {
      #pragma unroll
      for (unsigned r = 0; r < 4; ++r)
        if (r != role) part[n][r] = __builtin_bit_cast(f32x4, __builtin_amdgcn_raw_buffer_load_b128(srdS, lane * 16u, ((tile * 4u + r) * 4u + wid) * 18u * 1024u + i * 1024u, 16));
      xr[n] = *(const u32x2*)(x + (size_t)(tok0 + 16u * (i & 1u)) * DM + col0 + 16u * (i >> 1));
    }
  }
  #pragma unroll
  for (int n = 0; n < 5; ++n) {
    const unsigned i = role + 4u * n;
    if (i < 18u) {
      f32x4 own = c.acc[0][0];
      #pragma unroll
      for (int ii = 0; ii < 18; ++ii) if ((ii >> 2) == n && (unsigned)(ii & 3) == role) own = c.acc[ii >> 1][ii & 1];
      f32x4 pp[4];
      #pragma unroll
      for (unsigned r = 0; r < 4; ++r) pp[r] = (r == role) ? own : part[n][r];
      float o[4];
      #pragma unroll
      for (int e = 0; e < 4; ++e) {
        const float s = pp[0][e] + pp[1][e], f = pp[2][e] + pp[3][e];
        const float g = f * __builtin_amdgcn_rcpf(1.f + __builtin_amdgcn_exp2f(-1.4426950408889634f * f));
        const float xv = __uint_as_float((e & 1) ? (xr[n][e >> 1] & 0xffff0000u) : (xr[n][e >> 1] << 16));
        o[e] = xv + s * g;
      }
      u32x2 w = {cvtpk(o[0], o[1]), cvtpk(o[2], o[3])};
      *(u32x2*)(out + (size_t)(tok0 + 16u * (i & 1u)) * DM + col0 + 16u * (i >> 1)) = w;
    }
  }
}

extern "C" void kernel_launch(void* const* d_in, const int* in_sizes, int n_in, void* d_out, int out_size, void* d_ws, size_t ws_size, hipStream_t stream) {
  const unsigned short* x  = (const unsigned short*)d_in[0];
  const unsigned short* kc = (const unsigned short*)d_in[1];
  const unsigned short* vc = (const unsigned short*)d_in[2];
  const unsigned short* wq = (const unsigned short*)d_in[3];
  const unsigned short* wo = (const unsigned short*)d_in[4];
  const unsigned short* wg = (const unsigned short*)d_in[6];
  const unsigned short* wu = (const unsigned short*)d_in[7];
  const unsigned short* wd = (const unsigned short*)d_in[8];
  char* ws = (char*)d_ws;
  (void)hipFuncSetAttribute((const void*)k2_main, hipFuncAttributeMaxDynamicSharedMemorySize, (int)SHM_K2);
  (void)hipFuncSetAttribute((const void*)k3_out, hipFuncAttributeMaxDynamicSharedMemorySize, (int)SHM_K3);
  k1_layernorm<<<256, 256, 0, stream>>>(x, (unsigned short*)(ws + WS_XN), (unsigned*)(ws + WS_FLAG));
  k2_main<<<256, 256, SHM_K2, stream>>>(kc, vc, wq, wg, wu, (const unsigned short*)(ws + WS_XN), (unsigned short*)(ws + WS_H), (unsigned short*)(ws + WS_O));
  k3_out<<<256, 256, SHM_K3, stream>>>(x, wo, wd, (const unsigned short*)(ws + WS_O), (const unsigned short*)(ws + WS_H), (float*)(ws + WS_SLAB), (unsigned*)(ws + WS_FLAG), (unsigned short*)d_out);
}
```

```cpp
#include <hip/hip_runtime.h>
#include <hip/hip_bf16.h>
#include <utility>

typedef float    f32x16 __attribute__((ext_vector_type(16)));
typedef float    f32x2  __attribute__((ext_vector_type(2)));
typedef float    f32x4  __attribute__((ext_vector_type(4)));
typedef __bf16   bf16x8 __attribute__((ext_vector_type(8)));
typedef __bf16   bf16x4 __attribute__((ext_vector_type(4)));
typedef __bf16   bf16x2 __attribute__((ext_vector_type(2)));
typedef unsigned u32x4  __attribute__((ext_vector_type(4)));
typedef unsigned u32x2  __attribute__((ext_vector_type(2)));
template <int... Is> using iseq = std::integer_sequence<int, Is...>;
template <int N> using make_iseq = std::make_integer_sequence<int, N>;
typedef __amdgpu_buffer_rsrc_t srd_t;

constexpr int SEQ = 2048, DM = 576, NH = 32, DH = 128, FF = 4608;
constexpr size_t WS_XN = 0;
constexpr size_t WS_H = 4u << 20;
constexpr size_t WS_O = 24u << 20;
constexpr size_t WS_SLAB = 48u << 20;
constexpr size_t WS_FLAG = 72u << 20;

#define ST_DECL
#define ST(i)
#define STR(i)
#define ST_OUT(base)
#define SRD_WORD3 0x00020000
#define DI __device__ __forceinline__
#define GAP() __builtin_amdgcn_sched_barrier(0)
#define ACLOB_LO "a0","a1","a2","a3","a4","a5","a6","a7","a8","a9","a10","a11","a12","a13","a14","a15","a16","a17","a18","a19","a20","a21","a22","a23","a24","a25","a26","a27","a28","a29","a30","a31","a32","a33","a34","a35","a36","a37","a38","a39","a40","a41","a42","a43","a44","a45","a46","a47","a48","a49","a50","a51","a52","a53","a54","a55","a56","a57","a58","a59","a60","a61","a62","a63","a64","a65","a66","a67","a68","a69","a70","a71","a72","a73","a74","a75","a76","a77","a78","a79","a80","a81","a82","a83","a84","a85","a86","a87","a88","a89","a90","a91","a92","a93","a94","a95","a96","a97","a98","a99","a100","a101","a102","a103","a104","a105","a106","a107","a108","a109","a110","a111","a112","a113","a114","a115","a116","a117","a118","a119","a120","a121","a122","a123","a124","a125","a126","a127"
#define ACLOB_HI "a128","a129","a130","a131","a132","a133","a134","a135","a136","a137","a138","a139","a140","a141","a142","a143","a144","a145","a146","a147","a148","a149","a150","a151","a152","a153","a154","a155","a156","a157","a158","a159","a160","a161","a162","a163","a164","a165","a166","a167","a168","a169","a170","a171","a172","a173","a174","a175","a176","a177","a178","a179","a180","a181","a182","a183","a184","a185","a186","a187","a188","a189","a190","a191","a192","a193","a194","a195","a196","a197","a198","a199","a200","a201","a202","a203","a204","a205","a206","a207","a208","a209","a210","a211","a212","a213","a214","a215","a216","a217","a218","a219","a220","a221","a222","a223","a224","a225","a226","a227","a228","a229","a230","a231","a232","a233","a234","a235","a236","a237","a238","a239","a240","a241","a242","a243","a244","a245","a246","a247","a248","a249","a250","a251","a252","a253","a254","a255"

DI unsigned rfl(unsigned x) { return __builtin_amdgcn_readfirstlane(x); }
DI unsigned cvtpk(float lo, float hi) { f32x2 v = {lo, hi}; bf16x2 b = __builtin_convertvector(v, bf16x2); return __builtin_bit_cast(unsigned, b); }
DI float max3f(float a, float b, float c) { float r; asm("v_max3_f32 %0, %1, %2, %3" : "=v"(r) : "v"(a), "v"(b), "v"(c)); return r; }
DI float max2f(float a, float b) { float r; asm("v_max_f32_e32 %0, %1, %2" : "=v"(r) : "v"(a), "v"(b)); return r; }
DI float fadd_s(float a, float b) { float r; asm("v_add_f32_e32 %0, %1, %2" : "=v"(r) : "v"(a), "v"(b)); return r; }
DI float fmul_s(float a, float b) { float r; asm("v_mul_f32_e32 %0, %1, %2" : "=v"(r) : "v"(a), "v"(b)); return r; }
DI float fsub_s(float a, float b) { float r; asm("v_sub_f32_e32 %0, %1, %2" : "=v"(r) : "v"(a), "v"(b)); return r; }
DI float ffma_s(float a, float b, float c) { float r; asm("v_fma_f32 %0, %1, %2, %3" : "=v"(r) : "v"(a), "v"(b), "v"(c)); return r; }
DI srd_t mk_srd(const void* p, unsigned bytes) { return __builtin_amdgcn_make_buffer_rsrc((void*)p, (short)0, (int)bytes, SRD_WORD3); }
template <int R> DI float acc_rd() { float r; asm volatile("v_accvgpr_read_b32 %0, a[%c1]" : "=v"(r) : "i"(R)); return r; }
template <int R> DI void acc_wr(unsigned w) { asm volatile("v_accvgpr_write_b32 a[%c1], %0" :: "v"(w), "i"(R) : ACLOB_LO, ACLOB_HI); }
template <int N> DI void wait_vm() { asm volatile("s_waitcnt vmcnt(%c0)" :: "i"(N) : "memory"); }
DI void dma16(unsigned m0v, unsigned voff, srd_t srd, unsigned soff) {
  asm volatile("s_mov_b32 m0, %0\n\ts_nop 0\n\tbuffer_load_dwordx4 %1, %2, %3 offen lds" :: "s"(m0v), "v"(voff), "s"(srd), "s"(soff) : "m0", "memory");
}

__global__ __launch_bounds__(256) void k1_layernorm(const unsigned short* __restrict__ x, unsigned short* __restrict__ xn, unsigned* __restrict__ flags,
                                                    const unsigned short* __restrict__ Wg, const unsigned short* __restrict__ Wu, const unsigned short* __restrict__ Wq) {
  __shared__ __attribute__((aligned(16))) unsigned long long stg[72 * 16];
  const unsigned tid = threadIdx.x, l = tid & 31u, tok = tid >> 5;
  if (blockIdx.x == 0) flags[tid] = 0u;
  const unsigned t = blockIdx.x * 8u + tok;
  const u32x4* row = (const u32x4*)(x + (size_t)t * DM);
  u32x4 v[3];
  v[0] = row[l]; v[1] = row[l + 32]; v[2] = (l < 8u) ? row[l + 64] : (u32x4){0u, 0u, 0u, 0u};
  unsigned pf[6];
  { const srd_t sg = mk_srd(Wg, DM * FF * 2u), su = mk_srd(Wu, DM * FF * 2u), sq = mk_srd(Wq, DM * 4096u * 2u);
    const unsigned g = (blockIdx.x * 256u + tid) * 64u;
    GAP();
    pf[0] = __builtin_amdgcn_raw_buffer_load_b32(sg, g, 0, 0); pf[1] = __builtin_amdgcn_raw_buffer_load_b32(su, g, 0, 0); pf[2] = __builtin_amdgcn_raw_buffer_load_b32(sq, g, 0, 0);
    pf[3] = __builtin_amdgcn_raw_buffer_load_b32(sg, g, 65536u * 64u, 0); pf[4] = __builtin_amdgcn_raw_buffer_load_b32(su, g, 65536u * 64u, 0); pf[5] = __builtin_amdgcn_raw_buffer_load_b32(sq, g, 65536u * 64u, 0);
    GAP(); }
  float f[3][8]; float s = 0.f;
  #pragma unroll
  for (int c = 0; c < 3; ++c)
    #pragma unroll
    for (int e = 0; e < 4; ++e) { f[c][2 * e] = __uint_as_float(v[c][e] << 16); f[c][2 * e + 1] = __uint_as_float(v[c][e] & 0xffff0000u); s += f[c][2 * e] + f[c][2 * e + 1]; }
  #pragma unroll
  for (int m = 16; m >= 1; m >>= 1) s += __shfl_xor(s, m, 32);
  const float mean = s * (1.f / DM);
  float q = 0.f;
  #pragma unroll
  for (int c = 0; c < 3; ++c)
    #pragma unroll
    for (int e = 0; e < 8; ++e) { const float d = f[c][e] - mean; f[c][e] = d; if (c < 2 || l < 8u) q += d * d; }
  #pragma unroll
  for (int m = 16; m >= 1; m >>= 1) q += __shfl_xor(q, m, 32);
  const float rstd = 1.f / sqrtf(q * (1.f / DM) + 1e-5f);
  #pragma unroll
  for (int c = 0; c < 3; ++c) {
    if (c < 2 || l < 8u) {
      const unsigned m = l + 32u * c, sstep = m >> 1, jj = m & 1u;
      const unsigned long long lo = (unsigned long long)cvtpk(f[c][0] * rstd, f[c][1] * rstd) | ((unsigned long long)cvtpk(f[c][2] * rstd, f[c][3] * rstd) << 32);
      const unsigned long long hi = (unsigned long long)cvtpk(f[c][4] * rstd, f[c][5] * rstd) | ((unsigned long long)cvtpk(f[c][6] * rstd, f[c][7] * rstd) << 32);
      stg[(2u * sstep) * 16u + tok * 2u + jj] = lo;
      stg[(2u * sstep + 1u) * 16u + tok * 2u + jj] = hi;
    }
  }
  __syncthreads();
  const unsigned tb = blockIdx.x >> 2, tbase = (blockIdx.x & 3u) * 8u;
  for (unsigned u = tid; u < 576u; u += 256u) {
    const unsigned line = u >> 3, tk = u & 7u;
    const u32x4 w = *(const u32x4*)&stg[line * 16u + tk * 2u];
    *(u32x4*)((char*)xn + ((size_t)(tb * 36u + (line >> 1)) * 1024u + (line & 1u) * 512u + (tbase + tk) * 16u)) = w;
  }
  asm volatile("" :: "v"(pf[0]), "v"(pf[1]), "v"(pf[2]), "v"(pf[3]), "v"(pf[4]), "v"(pf[5]));
}

#define KRING 0x0000u
#define VRING 0x8000u
#define WRING 0x10000u
constexpr unsigned WSLOT = 20480u;
constexpr size_t SHM_K2 = 0x10000 + 3 * 20480;
constexpr unsigned KRS = 256u, KTILEB = 64u * KRS;
constexpr int NT_KV = SEQ / 64;
constexpr float THR = 8.0f;

constexpr int g_nt(int gc) { return gc / 9 == 1 ? 5 : 4; }
constexpr int g_ngaps(int gc) { return 8 * g_nt(gc); }
constexpr int EPI_OPS = 21;
constexpr int epi_units(int ph) { return ph == 1 ? 32 : (ph == 2 ? 40 : 0); }
constexpr int epi_lo(int ph, int G) { const int tg = 9 * 8 * (ph == 1 ? 5 : 4), to = epi_units(ph) * EPI_OPS; return G >= tg ? to : (int)(((long)G * to + tg - 1) / tg); }
constexpr int epi_stores(int gc, int M) {
  const int ph = gc / 9; if (ph == 0) return 0;
  const int G = (gc % 9) * g_ngaps(gc) + M; int n = 0;
  for (int o = epi_lo(ph, G); o < epi_lo(ph, G + 1); ++o) if (o % EPI_OPS == EPI_OPS - 1 && (o / EPI_OPS) % 4 == 3) ++n;
  return n;
}
constexpr int g_npiece(int gc) { return gc > 26 ? 0 : (gc / 9 == 1 ? 5 : 4); }
constexpr int g_xload(int gc, int M) { const int nt = g_nt(gc), ks = M / (2 * nt), i = M % (2 * nt); return (gc + 1 <= 26 && ks < 2 && i >= nt && i < nt + 4) ? 4 * ks + (i - nt) : -1; }
constexpr int g_wpiece(int gc, int M) { const int nt = g_nt(gc), ks = M / (2 * nt), i = M % (2 * nt), p = g_npiece(gc + 2); return (p > 0 && ks == 2 && i >= 2 * nt - p) ? i - (2 * nt - p) : -1; }
constexpr int g_kvpiece(int gc, int M) {
  const int ks = M / 8, i = M % 8;
  if (gc == 25 && ks == 2 && i >= 4) return i - 4;
  if (gc == 26 && ks < 2 && i >= 4) return 4 + 4 * ks + (i - 4);
  return -1;
}
constexpr int g_nvm(int gc, int M) { return (g_xload(gc, M) >= 0) + (g_wpiece(gc, M) >= 0) + (g_kvpiece(gc, M) >= 0) + epi_stores(gc, M); }
constexpr int g_vm_range(int gc, int m0, int m1) { int n = 0; for (int m = m0; m < m1; ++m) n += g_nvm(gc, m); return n; }
constexpr int g_wait_bar(int gc) { const int nt = g_nt(gc); return g_vm_range(gc, 0, 6 * nt) + (gc > 0 ? g_vm_range(gc - 1, 6 * g_nt(gc - 1), 8 * g_nt(gc - 1)) : 0); }
constexpr int g_wait_end(int gc) { const int nt = g_nt(gc); return g_vm_range(gc, 2 * nt + nt + 4, 8 * nt); }

struct GC {
  unsigned trb[3];
  unsigned wsrc1, wsrc2[5], wsrc3, xvo[2], hvo;
  srd_t srdX, srdGU, srdWq, srdH, srdK, srdV;
  unsigned m0g1, m0g2, m0q, sof1, sof2, sofq, hso;
  unsigned vksrc[2], vvsrc, m0k, m0v, sofk, sofv;
  bf16x8 B[2][4][2];
  bf16x4 alo[2][5], aup[2][5];
  bf16x8 A[2][5];
  f32x16 accV[2];
  float eg[2], eu[2], et[2], egu[2], kc0, kc1; u32x4 hw;
};

template <int PH, int T, int R> DI float epi_src(GC& c) {
  if constexpr (PH == 1) return acc_rd<16 * T + R>();
  else if constexpr (T < 8) return acc_rd<128 + 16 * T + R>();
  else return c.accV[T - 8][R];
}
template <int PH, int O> DI void epi_op(GC& c) {
  constexpr int u = O / EPI_OPS, o = O % EPI_OPS, T = u >> 2, pr = u & 3;
  if constexpr (o == 0) c.eg[0] = epi_src<PH, T, 2 * pr>(c);
  if constexpr (o == 1) c.eg[1] = epi_src<PH, T, 2 * pr + 1>(c);
  if constexpr (o == 2) c.eu[0] = epi_src<PH, T, 8 + 2 * pr>(c);
  if constexpr (o == 3) c.eu[1] = epi_src<PH, T, 8 + 2 * pr + 1>(c);
  if constexpr (o == 4 || o == 5)   asm volatile("v_mul_f32_e32 %0, %1, %1" : "=v"(c.et[o - 4]) : "v"(c.eg[o - 4]));
  if constexpr (o == 6 || o == 7)   asm volatile("v_fma_f32 %0, %0, %1, %2" : "+v"(c.et[o - 6]) : "v"(c.kc1), "v"(c.kc0));
  if constexpr (o == 8 || o == 9)   asm volatile("v_mul_f32_e32 %0, %0, %1" : "+v"(c.et[o - 8]) : "v"(c.eg[o - 8]));
  if constexpr (o == 10)            asm volatile("v_exp_f32_e32 %0, %0" : "+v"(c.et[0]));
  if constexpr (o == 11)            asm volatile("v_mul_f32_e32 %0, %1, %2" : "=v"(c.egu[0]) : "v"(c.eg[0]), "v"(c.eu[0]));
  if constexpr (o == 12)            asm volatile("v_exp_f32_e32 %0, %0" : "+v"(c.et[1]));
  if constexpr (o == 13)            asm volatile("v_mul_f32_e32 %0, %1, %2" : "=v"(c.egu[1]) : "v"(c.eg[1]), "v"(c.eu[1]));
  if constexpr (o == 14 || o == 15) asm volatile("v_add_f32_e32 %0, 1.0, %0" : "+v"(c.et[o - 14]));
  if constexpr (o == 16 || o == 17) asm volatile("v_rcp_f32_e32 %0, %0" : "+v"(c.et[o - 16]));
  if constexpr (o == 18 || o == 19) asm volatile("v_mul_f32_e32 %0, %0, %1" : "+v"(c.egu[o - 18]) : "v"(c.et[o - 18]));
  if constexpr (o == 20) {
    unsigned w; asm volatile("v_cvt_pk_bf16_f32 %0, %1, %2" : "=v"(w) : "v"(c.egu[0]), "v"(c.egu[1]));
    c.hw[pr] = w;
    if constexpr (pr == 3) {
      constexpr int ctg = (PH == 1 ? 0 : 4) + (T >> 1), qb = T & 1;
      __builtin_amdgcn_raw_buffer_store_b128(c.hw, c.srdH, c.hvo, c.hso + (unsigned)(qb * 288 + ctg) * 1024u, 0);
    }
  }
}
template <int PH, int O0, int... Is> DI void epi_ops(GC& c, iseq<Is...>) { (epi_op<PH, O0 + Is>(c), ...); }

template <int GCI, int KS, int CT, int HALF> DI void g_trread(GC& c) {
  constexpr int ph = GCI / 9, nt = g_nt(GCI), slot = GCI % 3, par = KS & 1;
  constexpr int off = slot * (int)WSLOT + (ph == 2 ? 4096 * KS + 2048 * HALF + 512 * CT : ((2 * KS + HALF) * nt + CT) * 256);
  if constexpr (HALF == 0) asm volatile("ds_read_b64_tr_b16 %0, %1 offset:%c2" : "=v"(c.alo[par][CT]) : "v"(c.trb[ph]), "i"(off) : "memory");
  else                     asm volatile("ds_read_b64_tr_b16 %0, %1 offset:%c2" : "=v"(c.aup[par][CT]) : "v"(c.trb[ph]), "i"(off) : "memory");
}
template <int GCI, int X> DI void g_xld(GC& c) {
  constexpr int ks = X >> 1, qb = X & 1;
  asm volatile("buffer_load_dwordx4 %0, %1, %2, %3 offen offset:%c4" : "=v"(c.B[GCI & 1][ks][qb]) : "v"(c.xvo[qb]), "s"(c.srdX), "s"((unsigned)((GCI % 9) * 4096)), "i"(ks * 1024) : "memory");
}
template <int GCI, int P> DI void g_wdma(GC& c, unsigned ldsw) {
  constexpr int ph = GCI / 9, cc = GCI % 9; constexpr unsigned slot = (GCI % 3) * WSLOT;
  if constexpr (ph == 0)      dma16(c.m0g1 + slot + 1024u * P, c.wsrc1, c.srdGU, c.sof1 + (unsigned)(cc * 64 + 8 * P) * (FF * 2u));
  else if constexpr (ph == 1) dma16(c.m0g2 + slot + 1024u * P, c.wsrc2[P], c.srdGU, c.sof2 + (unsigned)(cc * 64) * (FF * 2u));
  else                        dma16(c.m0q + slot + 1024u * P, c.wsrc3, c.srdWq, c.sofq + (unsigned)(cc * 64 + 8 * (P >> 1)) * 8192u + 128u * (P & 1));
}
template <int P> DI void g_kvdma(GC& c) {
  constexpr int q = P & 3;
  if constexpr (P < 4)      dma16(c.m0k + 1024u * q, c.vksrc[q & 1], c.srdK, c.sofk + KRS * 4u * q);
  else if constexpr (P < 8) dma16(c.m0v + 1024u * q, c.vvsrc, c.srdV, c.sofv + KRS * 8u * (q >> 1) + 128u * (q & 1));
  else                      dma16(c.m0k + 0x4000u + 1024u * q, c.vksrc[q & 1], c.srdK, KTILEB + c.sofk + KRS * 4u * q);
}
template <int GCI, int M> DI void g_gap(GC& c) {
  constexpr int ph = GCI / 9, cc = GCI % 9, nt = g_nt(GCI), ks = M / (2 * nt), i = M % (2 * nt), ct = i >> 1, qb = i & 1;
  constexpr bool Z = (cc == 0 && ks == 0);
  if constexpr (ph == 1 && ct == 4) {
    if constexpr (Z) asm volatile("v_mfma_f32_32x32x16_bf16 %0, %1, %2, 0" : "=&v"(c.accV[qb]) : "v"(c.A[ks & 1][ct]), "v"(c.B[GCI & 1][ks][qb]));
    else             asm volatile("v_mfma_f32_32x32x16_bf16 %0, %1, %2, %0" : "+v"(c.accV[qb]) : "v"(c.A[ks & 1][ct]), "v"(c.B[GCI & 1][ks][qb]));
  } else {
    constexpr int ao = (ph == 1 ? 128 : 0) + 16 * (2 * ct + qb);
    if constexpr (Z) asm volatile("v_mfma_f32_32x32x16_bf16 a[%c2:%c3], %0, %1, 0" :: "v"(c.A[ks & 1][ct]), "v"(c.B[GCI & 1][ks][qb]), "i"(ao), "i"(ao + 15) : ACLOB_LO, ACLOB_HI);
    else             asm volatile("v_mfma_f32_32x32x16_bf16 a[%c2:%c3], %0, %1, a[%c2:%c3]" :: "v"(c.A[ks & 1][ct]), "v"(c.B[GCI & 1][ks][qb]), "i"(ao), "i"(ao + 15) : ACLOB_LO, ACLOB_HI);
  }
  constexpr int ngc = ks == 3 ? GCI + 1 : GCI, nks = (ks + 1) & 3;
  if constexpr (ngc <= 26 && i < g_nt(ngc)) { g_trread<ngc, nks, i, 0>(c); g_trread<ngc, nks, i, 1>(c); }
  if constexpr (g_xload(GCI, M) >= 0) g_xld<GCI + 1, g_xload(GCI, M)>(c);
  if constexpr (g_wpiece(GCI, M) >= 0) g_wdma<GCI + 2, g_wpiece(GCI, M)>(c, 0u);
  if constexpr (g_kvpiece(GCI, M) >= 0) g_kvdma<g_kvpiece(GCI, M)>(c);
  if constexpr (ph > 0) { constexpr int G = cc * g_ngaps(GCI) + M, o0 = epi_lo(ph, G), o1 = epi_lo(ph, G + 1); epi_ops<ph, o0>(c, make_iseq<o1 - o0>{}); }
  GAP();
}
#define G_A5(p) "+v"(c.alo[p][0]), "+v"(c.alo[p][1]), "+v"(c.alo[p][2]), "+v"(c.alo[p][3]), "+v"(c.alo[p][4]), "+v"(c.aup[p][0]), "+v"(c.aup[p][1]), "+v"(c.aup[p][2]), "+v"(c.aup[p][3]), "+v"(c.aup[p][4])
#define G_A4(p) "+v"(c.alo[p][0]), "+v"(c.alo[p][1]), "+v"(c.alo[p][2]), "+v"(c.alo[p][3]), "+v"(c.aup[p][0]), "+v"(c.aup[p][1]), "+v"(c.aup[p][2]), "+v"(c.aup[p][3])
#define G_B8(b) "+v"(c.B[b][0][0]), "+v"(c.B[b][0][1]), "+v"(c.B[b][1][0]), "+v"(c.B[b][1][1]), "+v"(c.B[b][2][0]), "+v"(c.B[b][2][1]), "+v"(c.B[b][3][0]), "+v"(c.B[b][3][1])
template <int NTN, int PAR> DI void g_land(GC& c) {
  if constexpr (NTN == 5) asm volatile("s_waitcnt lgkmcnt(0)" : G_A5(PAR) :: "memory"); else asm volatile("s_waitcnt lgkmcnt(0)" : G_A4(PAR) :: "memory");
  GAP();
  #pragma unroll
  for (int ct = 0; ct < NTN; ++ct) c.A[PAR][ct] = __builtin_shufflevector(c.alo[PAR][ct], c.aup[PAR][ct], 0, 1, 2, 3, 4, 5, 6, 7);
  if constexpr (NTN == 5) asm volatile("s_nop 1" : "+v"(c.A[PAR][0]), "+v"(c.A[PAR][1]), "+v"(c.A[PAR][2]), "+v"(c.A[PAR][3]), "+v"(c.A[PAR][4]));
  else                    asm volatile("s_nop 1" : "+v"(c.A[PAR][0]), "+v"(c.A[PAR][1]), "+v"(c.A[PAR][2]), "+v"(c.A[PAR][3]));
  GAP();
}
template <int GCI, int M0, int... Is> DI void g_gaps(GC& c, iseq<Is...>) { (g_gap<GCI, M0 + Is>(c), ...); }
template <int GCI> DI void g_chunk(GC& c) {
  constexpr int nt = g_nt(GCI), n2 = 2 * nt;
  g_gaps<GCI, 0>(c, make_iseq<n2>{});       g_land<nt, 1>(c);
  g_gaps<GCI, n2>(c, make_iseq<n2>{});      g_land<nt, 0>(c);
  g_gaps<GCI, 2 * n2>(c, make_iseq<n2>{});  g_land<nt, 1>(c);
  if constexpr (GCI < 26) { wait_vm<g_wait_bar(GCI)>(); asm volatile("s_barrier" ::: "memory"); GAP(); }
  g_gaps<GCI, 3 * n2>(c, make_iseq<n2>{});
  if constexpr (GCI < 26) {
    asm volatile("s_waitcnt vmcnt(%c8)" : G_B8((GCI + 1) & 1) : "i"(g_wait_end(GCI)) : "memory");
    g_land<g_nt(GCI + 1), 0>(c);
  }
}
template <int G0, int... Gs> DI void g_chunks(GC& c, iseq<Gs...>) { (g_chunk<G0 + Gs>(c), ...); }
template <int... Ps> DI void g_wdma_all0(GC& c, iseq<Ps...>) { (g_wdma<0, Ps>(c, 0u), ...); }
template <int... Ps> DI void g_wdma_all1(GC& c, iseq<Ps...>) { (g_wdma<1, Ps>(c, 0u), ...); }
template <int... Xs> DI void g_xld_all0(GC& c, iseq<Xs...>) { (g_xld<0, Xs>(c), ...); }
template <int... Xs> DI void g_tr_all0(GC& c, iseq<Xs...>) { (g_trread<0, 0, (Xs >> 1), (Xs & 1)>(c), ...); }
template <int I> DI void q_pack(float sc) {
  constexpr int qb = I >> 5, d0 = (I >> 2) & 7, e = I & 3, src = 16 * (2 * (d0 >> 1) + qb) + 8 * (d0 & 1) + 2 * e;
  const float a = acc_rd<src>(), b = acc_rd<src + 1>();
  acc_wr<128 + 32 * qb + 4 * d0 + e>(cvtpk(a * sc, b * sc));
}
template <int... Is> DI void q_packs(float sc, iseq<Is...>) { (q_pack<Is>(sc), ...); }

DI float w4_aone() { unsigned l; asm volatile("v_mbcnt_lo_u32_b32 %0, -1, 0\n\tv_mbcnt_hi_u32_b32 %0, -1, %0" : "=v"(l)); return l < 32u ? 1.f : 0.f; }
DI void negm_bcast(f32x16& negm, float aone, float neg_mhat) { asm volatile("s_nop 1\n\tv_mfma_f32_32x32x2_f32 %0, %1, %2, 0" : "+v"(negm) : "v"(aone), "v"(neg_mhat)); }
struct W4C {
  unsigned vkb[4], vvb, vksrc[2], vvsrc, m0k, m0v, sofk, sofv;
  srd_t srdK, srdV, srdP; unsigned pfm0, pfvo, pfso;
  f32x16 S[2][2][2];
  f32x16 NEGM[2];
  bf16x8 Vf[4][4], Pk[2][4];
  bf16x4 vlo[4][4], vup[4][4];
  float mhat[2], lsum[2], fct[2]; bool pend;
  float rs0[2], rs1[2], pmx0[2], pmx1[2], rmax[2], delta[2];
  unsigned pm0[8], psf[8];
};
constexpr int AO_(int db, int qb) { return 16 * (2 * db + qb); }
constexpr int AQ_(int qb, int d0) { return 128 + 32 * qb + 4 * d0; }
constexpr int AK_(int kb, int d0) { return 192 + 32 * kb + 4 * d0; }
template <int I> DI void tr_read(W4C& c, unsigned vslot) {
  constexpr int ks = I >> 3, db = (I >> 1) & 3;
  if constexpr ((I & 1) == 0) asm volatile("ds_read_b64_tr_b16 %0, %1 offset:%2" : "=v"(c.vlo[db][ks]) : "v"(c.vvb + vslot), "i"(4096 * ks + 512 * db) : "memory");
  else                        asm volatile("ds_read_b64_tr_b16 %0, %1 offset:%2" : "=v"(c.vup[db][ks]) : "v"(c.vvb + vslot), "i"(4096 * ks + 2048 + 512 * db) : "memory");
}
template <int I> DI void k_read(W4C& c, unsigned slot) {
  constexpr int kb = I >> 3, d0 = I & 7;
  asm volatile("ds_read_b128 a[%c1:%c2], %0 offset:%c3" :: "v"(c.vkb[d0 & 3] + slot), "i"(AK_(kb, d0)), "i"(AK_(kb, d0) + 3), "i"(8192 * kb + 128 * (d0 >> 2)) : "memory", ACLOB_HI);
}
template <int X, bool FIRST, int M> DI void qk_mfma(W4C& c) {
  constexpr int d0 = M >> 2, kb = (M >> 1) & 1, qb = M & 1, ak = AK_(kb, d0), aq = AQ_(qb, d0);
  if constexpr (d0 == 0) {
    if constexpr (FIRST) asm volatile("v_mfma_f32_32x32x16_bf16 %0, a[%c1:%c2], a[%c3:%c4], 0" : "=&v"(c.S[X][kb][qb]) : "i"(ak), "i"(ak + 3), "i"(aq), "i"(aq + 3) : ACLOB_LO, ACLOB_HI);
    else                 asm volatile("v_mfma_f32_32x32x16_bf16 %0, a[%c2:%c3], a[%c4:%c5], %1" : "=&v"(c.S[X][kb][qb]) : "v"(c.NEGM[qb]), "i"(ak), "i"(ak + 3), "i"(aq), "i"(aq + 3) : ACLOB_LO, ACLOB_HI);
  } else asm volatile("v_mfma_f32_32x32x16_bf16 %0, a[%c1:%c2], a[%c3:%c4], %0" : "+v"(c.S[X][kb][qb]) : "i"(ak), "i"(ak + 3), "i"(aq), "i"(aq + 3) : ACLOB_LO, ACLOB_HI);
}
template <int M, bool ZERO> DI void pv_mfma(W4C& c) {
  constexpr int ks = M >> 3, db = (M >> 1) & 3, qb = M & 1, ao = AO_(db, qb);
  if constexpr (ZERO && ks == 0) asm volatile("v_mfma_f32_32x32x16_bf16 a[%c2:%c3], %0, %1, 0" :: "v"(c.Vf[db][ks]), "v"(c.Pk[qb][ks]), "i"(ao), "i"(ao + 15) : ACLOB_LO);
  else                           asm volatile("v_mfma_f32_32x32x16_bf16 a[%c2:%c3], %0, %1, a[%c2:%c3]" :: "v"(c.Vf[db][ks]), "v"(c.Pk[qb][ks]), "i"(ao), "i"(ao + 15) : ACLOB_LO);
}
template <int X, int QB, int KS, int P> DI void pack1(W4C& c) {
  u32x4 w = __builtin_bit_cast(u32x4, c.Pk[QB][KS]);
  w[P] = cvtpk(c.S[X][KS >> 1][QB][8 * (KS & 1) + 2 * P], c.S[X][KS >> 1][QB][8 * (KS & 1) + 2 * P + 1]);
  c.Pk[QB][KS] = __builtin_bit_cast(bf16x8, w);
}
template <int X, int J> DI void rowsum1(W4C& c) {
  constexpr int qb = J >> 4, ks = (J >> 2) & 3, p = J & 3;
  c.rs0[qb] = fadd_s(c.rs0[qb], c.S[X][ks >> 1][qb][8 * (ks & 1) + 2 * p]); c.rs1[qb] = fadd_s(c.rs1[qb], c.S[X][ks >> 1][qb][8 * (ks & 1) + 2 * p + 1]);
}
template <int X, bool FIRST, int M> DI void gapA(W4C& c, unsigned vslot, const int t) {
  constexpr int Xp = X ^ 1;
  qk_mfma<X, FIRST, M>(c);
  if constexpr (M >= 16 && M <= 31) {
    constexpr int q = (M - 16) >> 1;
    if constexpr (((M - 16) & 1) == 0) {
      if constexpr (q < 4) c.pm0[q] = c.m0k + (unsigned)((t + 2) & 1) * 0x4000u + 1024u * q; else c.pm0[q] = c.m0v + (unsigned)((t + 1) & 1) * 0x4000u + 1024u * (q - 4);
      asm volatile("" : "+s"(c.pm0[q]));
    } else {
      if constexpr (q < 4) c.psf[q] = KTILEB * (unsigned)(t + 2) + c.sofk + KRS * 4u * q; else c.psf[q] = KTILEB * (unsigned)(t + 1) + c.sofv + KRS * 8u * ((q - 4) >> 1) + 128u * ((q - 4) & 1);
      asm volatile("" : "+s"(c.psf[q]));
    }
  }
  if constexpr (!FIRST && M == 8) asm volatile("" :: "v"(c.NEGM[0]), "v"(c.NEGM[1]));
  if constexpr (!FIRST) {
    constexpr int base = M + (M >= 4) + (M >= 8) + (M >= 12) + (M >= 16) + (M >= 20) + (M >= 24);
    if constexpr (M <= 25) tr_read<base>(c, vslot);
    if constexpr (M == 3 || M == 7 || M == 11 || M == 15 || M == 19 || M == 23) tr_read<base + 1>(c, vslot);
    if constexpr (M < 16) {
      constexpr int e0 = 2 * M, e1 = 2 * M + 1;
      c.S[Xp][1][e0 >> 4][e0 & 15] = __builtin_amdgcn_exp2f(c.S[Xp][1][e0 >> 4][e0 & 15]);
      c.S[Xp][1][e1 >> 4][e1 & 15] = __builtin_amdgcn_exp2f(c.S[Xp][1][e1 >> 4][e1 & 15]);
      pack1<Xp, (M >> 3), ((M >> 2) & 1), (M & 3)>(c);
    } else {
      constexpr int cc = M - 16;
      pack1<Xp, (cc >> 3), 2 + ((cc >> 2) & 1), (cc & 3)>(c);
      rowsum1<Xp, 16 * (cc >> 3) + 8 + 4 * ((cc >> 2) & 1) + (cc & 3)>(c);
    }
  }
  GAP();
}
#define W4_VLO "+v"(c.vlo[0][0]), "+v"(c.vlo[0][1]), "+v"(c.vlo[0][2]), "+v"(c.vlo[0][3]), "+v"(c.vlo[1][0]), "+v"(c.vlo[1][1]), "+v"(c.vlo[1][2]), "+v"(c.vlo[1][3]), \
               "+v"(c.vlo[2][0]), "+v"(c.vlo[2][1]), "+v"(c.vlo[2][2]), "+v"(c.vlo[2][3]), "+v"(c.vlo[3][0]), "+v"(c.vlo[3][1]), "+v"(c.vlo[3][2]), "+v"(c.vlo[3][3])
#define W4_VUP "+v"(c.vup[0][0]), "+v"(c.vup[0][1]), "+v"(c.vup[0][2]), "+v"(c.vup[0][3]), "+v"(c.vup[1][0]), "+v"(c.vup[1][1]), "+v"(c.vup[1][2]), "+v"(c.vup[1][3]), \
               "+v"(c.vup[2][0]), "+v"(c.vup[2][1]), "+v"(c.vup[2][2]), "+v"(c.vup[2][3]), "+v"(c.vup[3][0]), "+v"(c.vup[3][1]), "+v"(c.vup[3][2]), "+v"(c.vup[3][3])
template <int X, bool FIRST, int... Ms> DI void phaseA_(W4C& c, const int t, iseq<Ms...>) {
  const unsigned vslot = (unsigned)((t - 1) & 1) * 0x4000u;
  (gapA<X, FIRST, Ms>(c, vslot, t), ...);
  if constexpr (!FIRST) {
    #pragma unroll
    for (int qb = 0; qb < 2; ++qb) {
      const float rs = fadd_s(c.rs0[qb], c.rs1[qb]);
      auto pr = __builtin_amdgcn_permlane32_swap(__float_as_uint(rs), __float_as_uint(rs), false, false);
      c.lsum[qb] = fadd_s(c.lsum[qb], fadd_s(__uint_as_float(pr[0]), __uint_as_float(pr[1])));
    }
  }
  if constexpr (FIRST) asm volatile("s_waitcnt vmcnt(0) lgkmcnt(0)\n\ts_barrier" ::: "memory");
  else { asm volatile("s_waitcnt vmcnt(0) lgkmcnt(0)" : W4_VLO :: "memory"); asm volatile("s_barrier" : W4_VUP :: "memory"); }
  GAP();
  if constexpr (!FIRST) {
    #pragma unroll
    for (int ks = 0; ks < 4; ++ks)
      #pragma unroll
      for (int db = 0; db < 4; ++db) c.Vf[db][ks] = __builtin_shufflevector(c.vlo[db][ks], c.vup[db][ks], 0, 1, 2, 3, 4, 5, 6, 7);
  }
}
template <int X, bool FIRST> DI void phaseA(W4C& c, const int t) { phaseA_<X, FIRST>(c, t, make_iseq<32>{}); }
template <int R> DI void o_scale1(float f0, float f1) {
  float t; asm volatile("v_accvgpr_read_b32 %0, a[%c2]\n\tv_mul_f32 %0, %1, %0\n\tv_accvgpr_write_b32 a[%c2], %0" : "=&v"(t) : "v"(((R >> 4) & 1) ? f1 : f0), "i"(R) : ACLOB_LO);
}
template <int... Rs> DI void o_scale(float f0, float f1, iseq<Rs...>) { (o_scale1<Rs>(f0, f1), ...); }
template <int X, bool PEEL, bool ZERO1, bool DK, bool DV, bool KR, int M> DI void gapB(W4C& c, const unsigned kslot_rd) {
  if constexpr (!PEEL) pv_mfma<M, ZERO1>(c);
  if constexpr (M < 4 && DK)                dma16(c.pm0[M], c.vksrc[M & 1], c.srdK, c.psf[M]);
  else if constexpr (M >= 4 && M < 8 && DV) dma16(c.pm0[M], c.vvsrc, c.srdV, c.psf[M]);
  if constexpr (M == 9 && DK) { dma16(c.pfm0, c.pfvo, c.srdP, c.pfso); c.pfso += 1024u; }
  if constexpr (M >= 1 && M <= 16 && KR) k_read<M - 1>(c, kslot_rd);
  if constexpr (M >= 8 && M < 16) {
    constexpr int i = M - 8, q = i >> 2, r = 4 * (i & 3);
    if constexpr ((i & 3) == 0) { c.pmx0[q] = max3f(c.S[X][0][q][0], c.S[X][0][q][1], c.S[X][1][q][0]); c.pmx1[q] = max3f(c.S[X][0][q][2], c.S[X][0][q][3], c.S[X][1][q][1]);
                                  c.pmx0[q] = max3f(c.pmx0[q], c.S[X][1][q][2], c.S[X][1][q][3]); }
    else                        { c.pmx0[q] = max3f(c.pmx0[q], c.S[X][0][q][r], c.S[X][0][q][r + 1]); c.pmx1[q] = max3f(c.pmx1[q], c.S[X][0][q][r + 2], c.S[X][0][q][r + 3]);
                                  c.pmx0[q] = max3f(c.pmx0[q], c.S[X][1][q][r], c.S[X][1][q][r + 1]); c.pmx1[q] = max3f(c.pmx1[q], c.S[X][1][q][r + 2], c.S[X][1][q][r + 3]); }
  }
  if constexpr (M == 16 || M == 17) {
    constexpr int q = M - 16; const float mm = max2f(c.pmx0[q], c.pmx1[q]);
    auto pr = __builtin_amdgcn_permlane32_swap(__float_as_uint(mm), __float_as_uint(mm), false, false);
    c.rmax[q] = max2f(__uint_as_float(pr[0]), __uint_as_float(pr[1]));
  }
  if constexpr (M == 18) {
    if constexpr (PEEL) {
      #pragma unroll
      for (int q = 0; q < 2; ++q) { c.mhat[q] = c.rmax[q]; c.lsum[q] = 0.f; c.fct[q] = 1.f;
        #pragma unroll
        for (int kb = 0; kb < 2; ++kb) { _Pragma("unroll") for (int e = 0; e < 16; ++e) c.S[X][kb][q][e] = fsub_s(c.S[X][kb][q][e], c.rmax[q]); }
        negm_bcast(c.NEGM[q], w4_aone(), -c.mhat[q]);
      }
    } else {
      const bool grew = (c.rmax[0] > THR) | (c.rmax[1] > THR);
      if (__builtin_expect(__any((int)grew), 0)) {
        #pragma unroll
        for (int q = 0; q < 2; ++q) { const float dl = max2f(c.rmax[q], 0.f); c.delta[q] = dl; c.mhat[q] = fadd_s(c.mhat[q], dl); c.fct[q] = __builtin_amdgcn_exp2f(-dl);
          #pragma unroll
          for (int kb = 0; kb < 2; ++kb) { _Pragma("unroll") for (int e = 0; e < 16; ++e) c.S[X][kb][q][e] = fsub_s(c.S[X][kb][q][e], dl); }
          negm_bcast(c.NEGM[q], w4_aone(), -c.mhat[q]);
        }
        c.pend = true;
      }
    }
  }
  if constexpr (M == 18) { c.rs0[0] = c.rs0[1] = c.rs1[0] = c.rs1[1] = 0.f; }
  if constexpr (M >= 19) {
    constexpr int g = M - 19, e_lo = (32 * g) / 13, e_hi = (32 * (g + 1)) / 13;
    {
      constexpr int p_lo = g ? (32 * (g - 1)) / 13 : 0, p_hi = g ? (32 * g) / 13 : 0;
      if constexpr (p_lo + 0 < p_hi) { constexpr int e = p_lo + 0; if constexpr (e & 1) c.rs1[e >> 4] = fadd_s(c.rs1[e >> 4], c.S[X][0][e >> 4][e & 15]); else c.rs0[e >> 4] = fadd_s(c.rs0[e >> 4], c.S[X][0][e >> 4][e & 15]); }
      if constexpr (p_lo + 1 < p_hi) { constexpr int e = p_lo + 1; if constexpr (e & 1) c.rs1[e >> 4] = fadd_s(c.rs1[e >> 4], c.S[X][0][e >> 4][e & 15]); else c.rs0[e >> 4] = fadd_s(c.rs0[e >> 4], c.S[X][0][e >> 4][e & 15]); }
      if constexpr (p_lo + 2 < p_hi) { constexpr int e = p_lo + 2; if constexpr (e & 1) c.rs1[e >> 4] = fadd_s(c.rs1[e >> 4], c.S[X][0][e >> 4][e & 15]); else c.rs0[e >> 4] = fadd_s(c.rs0[e >> 4], c.S[X][0][e >> 4][e & 15]); }
    }
    if constexpr (e_lo + 0 < e_hi) c.S[X][0][(e_lo + 0) >> 4][(e_lo + 0) & 15] = __builtin_amdgcn_exp2f(c.S[X][0][(e_lo + 0) >> 4][(e_lo + 0) & 15]);
    if constexpr (e_lo + 1 < e_hi) c.S[X][0][(e_lo + 1) >> 4][(e_lo + 1) & 15] = __builtin_amdgcn_exp2f(c.S[X][0][(e_lo + 1) >> 4][(e_lo + 1) & 15]);
    if constexpr (e_lo + 2 < e_hi) c.S[X][0][(e_lo + 2) >> 4][(e_lo + 2) & 15] = __builtin_amdgcn_exp2f(c.S[X][0][(e_lo + 2) >> 4][(e_lo + 2) & 15]);
  }
  if constexpr (M == 31) asm volatile("" :: "v"(c.S[X][0][0]), "v"(c.S[X][0][1]), "v"(c.NEGM[0]), "v"(c.NEGM[1]), "v"(c.rs0[0]), "v"(c.rs0[1]), "v"(c.rs1[0]), "v"(c.rs1[1]));
  GAP();
}
template <int X, bool PEEL, bool ZERO1, bool DK, bool DV, bool KR, int... Ms> DI void phaseB_(W4C& c, const int t, iseq<Ms...>) {
  const unsigned kslot_rd = (unsigned)((t + 1) & 1) * 0x4000u;
  if constexpr (!PEEL) asm volatile("s_nop 1" ::: "memory");
  GAP();
  (gapB<X, PEEL, ZERO1, DK, DV, KR, Ms>(c, kslot_rd), ...);
  {
    #pragma unroll
    for (int e = (32 * 12) / 13; e < 32; ++e) { if (e & 1) c.rs1[e >> 4] = fadd_s(c.rs1[e >> 4], c.S[X][0][e >> 4][e & 15]); else c.rs0[e >> 4] = fadd_s(c.rs0[e >> 4], c.S[X][0][e >> 4][e & 15]); }
  }
  if constexpr (!PEEL) {
    if (__builtin_expect(c.pend, 0)) {
      asm volatile("s_nop 7\n\ts_nop 7" ::: "memory");
      o_scale(c.fct[0], c.fct[1], make_iseq<128>{});
      asm volatile("s_nop 1" ::: "memory");
      c.lsum[0] = fmul_s(c.lsum[0], c.fct[0]); c.lsum[1] = fmul_s(c.lsum[1], c.fct[1]); c.pend = false;
    }
  }
  asm volatile("s_waitcnt lgkmcnt(0)" ::: "memory");
  GAP();
}
template <int X, bool PEEL, bool ZERO1, bool DK = true, bool DV = true, bool KR = true> DI void phaseB(W4C& c, const int t) { phaseB_<X, PEEL, ZERO1, DK, DV, KR>(c, t, make_iseq<32>{}); }
template <int... Is> DI void k_reads(W4C& c, unsigned slot, iseq<Is...>) { (k_read<Is>(c, slot), ...); }
template <int... Is> DI void tr_reads(W4C& c, unsigned vslot, iseq<Is...>) { (tr_read<Is>(c, vslot), ...); }
template <int X, int... Cs> DI void packs(W4C& c, iseq<Cs...>) { (pack1<X, ((Cs >> 3) & 1), 2 * (Cs >> 4) + ((Cs >> 2) & 1), (Cs & 3)>(c), ...); }
template <int X, int J> DI void rowsum1_padded(W4C& c) { rowsum1<X, J>(c); asm volatile("s_nop 1"); }
template <int X, int... Js> DI void rowsums(W4C& c, iseq<Js...>) { (rowsum1_padded<X, Js>(c), ...); }
template <int... Ms> DI void pv_all(W4C& c, iseq<Ms...>) { (pv_mfma<Ms, false>(c), ...); }
template <int F> DI void o_store(float il, srd_t srdO, unsigned voff, unsigned soff) {
  constexpr int qb = F >> 3, st = F & 7, src = 16 * (2 * (st >> 1) + qb) + 8 * (st & 1);
  u32x4 w;
  w[0] = cvtpk(acc_rd<src + 0>() * il, acc_rd<src + 1>() * il); w[1] = cvtpk(acc_rd<src + 2>() * il, acc_rd<src + 3>() * il);
  w[2] = cvtpk(acc_rd<src + 4>() * il, acc_rd<src + 5>() * il); w[3] = cvtpk(acc_rd<src + 6>() * il, acc_rd<src + 7>() * il);
  __builtin_amdgcn_raw_buffer_store_b128(w, srdO, voff, soff + (unsigned)(qb * 256 + st) * 1024u, 0);
}
template <int... Fs> DI void o_stores(float il0, float il1, srd_t srdO, unsigned voff, unsigned soff, iseq<Fs...>) { (o_store<Fs>((Fs >> 3) ? il1 : il0, srdO, voff, soff), ...); }

__global__ __launch_bounds__(256, 1)
void k2_main(const unsigned short* __restrict__ Kc, const unsigned short* __restrict__ Vc, const unsigned short* __restrict__ Wq,
             const unsigned short* __restrict__ Wg, const unsigned short* __restrict__ Wu,
             const unsigned short* __restrict__ xn, unsigned short* __restrict__ hbuf, unsigned short* __restrict__ obuf,
             const unsigned short* __restrict__ Wo, const unsigned short* __restrict__ Wd) {
  extern __shared__ __attribute__((aligned(16))) char lds[];
  const unsigned tid = threadIdx.x, lane = tid & 63u, l32 = lane & 31u, hi = lane >> 5;
  const unsigned wid = rfl(tid >> 6);
  const unsigned ldsb = (unsigned)(size_t)lds;
  const unsigned bx = blockIdx.x, idx = bx >> 3, j = (bx & 7u) * 4u + (idx & 3u), rb = idx >> 2;
  const unsigned tb0 = rb * 8u + 2u * wid;
  float lsum0, lsum1;
  ST_DECL; STR(8); ST(0);
  {
    GC g;
    const unsigned q4 = (lane >> 2) & 3u, p4 = lane & 3u, blk = (lane >> 4) & 1u, isU = wid >> 1, wl = wid & 1u;
    const unsigned rowu = (4u * hi + q4) ^ (4u * blk);
    g.trb[0] = ldsb + WRING + rowu * 32u + p4 * 8u + blk * 8192u;
    g.trb[1] = ldsb + WRING + rowu * 32u + p4 * 8u + blk * 10240u;
    g.trb[2] = ldsb + WRING + (blk << 5) + ((p4 & 1u) << 4) + ((p4 >> 1) << 3) + (q4 << 6) + (hi << 8);
    const unsigned rowx = ((lane >> 1) & 7u) ^ (4u * isU);
    g.wsrc1 = rowx * (FF * 2u) + (lane >> 4) * 32u + (lane & 1u) * 16u;
    #pragma unroll
    for (int p = 0; p < 5; ++p) { const unsigned u = 4u * p + (lane >> 4), rg = u / 5u, ct = u - 5u * rg; g.wsrc2[p] = (8u * rg + rowx) * (FF * 2u) + ct * 32u + (lane & 1u) * 16u; }
    g.wsrc3 = 8192u * ((lane >> 2) & 7u) + 64u * hi + 16u * (lane & 3u);
    g.xvo[0] = lane * 16u; g.xvo[1] = lane * 16u + 36864u; g.hvo = lane * 16u;
    g.srdX = mk_srd((const char*)xn + (size_t)tb0 * 36864u, 2u * 36864u);
    g.srdGU = mk_srd(isU ? Wu : Wg, DM * FF * 2u);
    g.srdWq = mk_srd(Wq, DM * NH * DH * 2u);
    g.srdH = mk_srd((char*)hbuf + (size_t)tb0 * 288u * 1024u, 2u * 288u * 1024u);
    g.srdK = mk_srd((const char*)Kc + (size_t)j * SEQ * 256u, SEQ * 256u);
    g.srdV = mk_srd((const char*)Vc + (size_t)j * SEQ * 256u, SEQ * 256u);
    g.m0g1 = ldsb + WRING + isU * 8192u + wl * 4096u;   g.sof1 = wl * 32u * (FF * 2u) + j * 288u;
    g.m0g2 = ldsb + WRING + isU * 10240u + wl * 5120u;  g.sof2 = wl * 32u * (FF * 2u) + j * 288u + 128u;
    g.m0q = ldsb + WRING + wid * 4096u;                 g.sofq = wid * 16u * 8192u + j * 256u;
    g.hso = j * 9u * 1024u;
    #pragma unroll
    for (int par = 0; par < 2; ++par) g.vksrc[par] = KRS * (lane >> 4) + 16u * ((lane & 15u) ^ (4u * par + (lane >> 4)));
    g.vvsrc = KRS * ((lane >> 2) & 7u) + 64u * hi + 16u * (lane & 3u);
    g.m0k = ldsb + KRING + 4096u * wid; g.m0v = ldsb + VRING + 4096u * wid; g.sofk = KRS * 16u * wid; g.sofv = KRS * 16u * wid;
    g.kc0 = -2.302208198f; g.kc1 = -0.1029432397f;
    asm volatile("" : "+v"(g.kc0), "+v"(g.kc1));
    asm volatile("s_nop 4" ::: "memory");
    g_wdma_all0(g, make_iseq<4>{}); g_xld_all0(g, make_iseq<8>{}); g_wdma_all1(g, make_iseq<4>{});
    { GC& c = g; asm volatile("s_waitcnt vmcnt(4)\n\ts_barrier" : G_B8(0) :: "memory"); }
    g_tr_all0(g, make_iseq<8>{});
    g_land<4, 0>(g);
    ST(1);
    g_chunks<0>(g, make_iseq<9>{});
    ST(2);
    g_chunks<9>(g, make_iseq<9>{});
    ST(3);
    g_chunks<18>(g, make_iseq<9>{});
    ST(4);
    asm volatile("s_nop 7\n\ts_nop 7" ::: "memory");
    q_packs(0.12751743082f, make_iseq<64>{});
  }
  W4C c;
  #pragma unroll
  for (int jj = 0; jj < 4; ++jj) c.vkb[jj] = ldsb + KRING + 256u * l32 + 16u * ((2u * jj + hi) ^ (l32 & 7u));
  c.vvb = ldsb + VRING + (((lane & 3u) << 3) | (((lane >> 2) & 3u) << 6) | (((lane >> 4) & 1u) << 5) | (hi << 8));
  #pragma unroll
  for (int par = 0; par < 2; ++par) c.vksrc[par] = KRS * (lane >> 4) + 16u * ((lane & 15u) ^ (4u * par + (lane >> 4)));
  c.vvsrc = KRS * ((lane >> 2) & 7u) + 64u * hi + 16u * (lane & 3u);
  c.m0k = ldsb + KRING + 4096u * wid; c.m0v = ldsb + VRING + 4096u * wid; c.sofk = KRS * 16u * wid; c.sofv = KRS * 16u * wid;
  c.srdK = mk_srd((const char*)Kc + (size_t)j * SEQ * 256u, SEQ * 256u);
  c.srdV = mk_srd((const char*)Vc + (size_t)j * SEQ * 256u, SEQ * 256u);
  {
    const unsigned g = bx * 4u + wid, isD = g >> 9, npc = isD ? 11u : 9u, p0 = (g & 511u) * npc * 1024u, tot = isD ? FF * DM * 2u : NH * DH * DM * 2u;
    c.srdP = mk_srd(isD ? Wd : Wo, (p0 + npc * 1024u < tot) ? p0 + npc * 1024u : tot);
    c.pfso = p0; c.pfvo = lane * 16u; c.pfm0 = ldsb + WRING + 1024u * wid;
  }
  asm volatile("s_waitcnt vmcnt(0)\n\ts_barrier" ::: "memory");
  k_reads(c, 0u, make_iseq<16>{});
  asm volatile("s_waitcnt lgkmcnt(0)\n\ts_nop 1" ::: "memory");
  ST(5);
  c.mhat[0] = c.mhat[1] = 0.f; c.lsum[0] = c.lsum[1] = 0.f; c.fct[0] = c.fct[1] = 1.f; c.pend = false;
  constexpr int T = NT_KV;
  phaseA<0, true>(c, 0);  phaseB<0, true,  false>(c, 0);
  phaseA<1, false>(c, 1); phaseB<1, false, true>(c, 1);
  for (int t = 2; t < T - 2; t += 2) {
    phaseA<0, false>(c, t);     phaseB<0, false, false>(c, t);
    phaseA<1, false>(c, t + 1); phaseB<1, false, false>(c, t + 1);
  }
  phaseA<0, false>(c, T - 2); phaseB<0, false, false, false, true, true>(c, T - 2);
  phaseA<1, false>(c, T - 1); phaseB<1, false, false, false, false, false>(c, T - 1);
  {
    const unsigned vslot = (unsigned)((T - 1) & 1) * 0x4000u;
    tr_reads(c, vslot, make_iseq<32>{});
    #pragma unroll
    for (int e = 0; e < 32; ++e) c.S[1][1][e >> 4][e & 15] = __builtin_amdgcn_exp2f(c.S[1][1][e >> 4][e & 15]);
    packs<1>(c, make_iseq<32>{});
    rowsums<1>(c, iseq<8, 9, 10, 11, 12, 13, 14, 15, 24, 25, 26, 27, 28, 29, 30, 31>{});
    #pragma unroll
    for (int qb = 0; qb < 2; ++qb) { const float rs = fadd_s(c.rs0[qb], c.rs1[qb]);
      auto pr = __builtin_amdgcn_permlane32_swap(__float_as_uint(rs), __float_as_uint(rs), false, false);
      c.lsum[qb] = fadd_s(c.lsum[qb], fadd_s(__uint_as_float(pr[0]), __uint_as_float(pr[1]))); }
    asm volatile("s_waitcnt lgkmcnt(0)" : W4_VLO :: "memory"); asm volatile("" : W4_VUP :: "memory");
    GAP();
    #pragma unroll
    for (int ks = 0; ks < 4; ++ks)
      #pragma unroll
      for (int db = 0; db < 4; ++db) c.Vf[db][ks] = __builtin_shufflevector(c.vlo[db][ks], c.vup[db][ks], 0, 1, 2, 3, 4, 5, 6, 7);
    asm volatile("s_nop 1" ::: "memory");
    pv_all(c, make_iseq<32>{});
    asm volatile("s_nop 7\n\ts_nop 7" ::: "memory");
  }
  lsum0 = c.lsum[0]; lsum1 = c.lsum[1];
  ST(6);
  {
    const srd_t srdO = mk_srd((char*)obuf + (size_t)tb0 * 256u * 1024u, 2u * 256u * 1024u);
    o_stores(__builtin_amdgcn_rcpf(lsum0), __builtin_amdgcn_rcpf(lsum1), srdO, lane * 16u, j * 8u * 1024u, make_iseq<16>{});
  }
  ST(7); STR(9);
  ST_OUT((char*)xn + (80u << 20));
}

constexpr unsigned K3SLOT = 36864u;
constexpr size_t SHM_K3 = 4 * 36864;
typedef __attribute__((address_space(1))) unsigned gu32;
struct K3C {
  unsigned trb, wsrc[9], bvo[2];
  srd_t srdW, srdA;
  unsigned m0w, sofw;
  bf16x8 B[4][4][2];
  bf16x4 alo[2][9], aup[2][9];
  bf16x8 A[2][9];
};

template <int ST, int CT, int HALF> DI void k3_tr(K3C& c, unsigned base) {
  constexpr int off = ((4 * ST + HALF) * 9 + CT) * 256, par = ST & 1;
  if constexpr (HALF == 0) asm volatile("ds_read_b64_tr_b16 %0, %1 offset:%c2" : "=v"(c.alo[par][CT]) : "v"(base), "i"(off) : "memory");
  else                     asm volatile("ds_read_b64_tr_b16 %0, %1 offset:%c2" : "=v"(c.aup[par][CT]) : "v"(base), "i"(off) : "memory");
}
template <int BUF, int X> DI void k3_bld(K3C& c, unsigned soff) {
  constexpr int st = X >> 1, tg = X & 1;
  asm volatile("buffer_load_dwordx4 %0, %1, %2, %3 offen offset:%c4" : "=v"(c.B[BUF][st][tg]) : "v"(c.bvo[tg]), "s"(c.srdA), "s"(soff + (st >> 1) * 4096u), "i"((st & 1) * 2048) : "memory");
}
template <int BUF, int ST, int M> DI void k3_gap(K3C& c, unsigned trcur, unsigned trnext, unsigned bsoff, unsigned wm0, unsigned wsoff) {
  constexpr int ct = M >> 1, tg = M & 1, par = ST & 1;
  if constexpr (tg == 0) {
    asm volatile("s_waitcnt lgkmcnt(15)" : "+v"(c.alo[par][ct]), "+v"(c.aup[par][ct]) :: "memory");
    c.A[par][ct] = __builtin_shufflevector(c.alo[par][ct], c.aup[par][ct], 0, 1, 2, 3, 4, 5, 6, 7);
  }
  asm volatile("v_mfma_f32_16x16x32_bf16 a[%c2:%c3], %0, %1, a[%c2:%c3]" :: "v"(c.A[par][ct]), "v"(c.B[BUF][ST][tg]), "i"(4 * M), "i"(4 * M + 3) : ACLOB_LO);
  k3_tr<(ST + 1) & 3, ct, tg>(c, ST == 3 ? trnext : trcur);
  { constexpr int G = ST * 18 + M;
    if constexpr (G < 48 && G % 6 == 2) k3_bld<(BUF + 3) & 3, G / 6>(c, bsoff);
    if constexpr (G >= 55 && (G & 1) == 1) dma16(wm0 + 1024u * ((G - 55) >> 1), c.wsrc[(G - 55) >> 1], c.srdW, wsoff); }
  GAP();
}
template <int BUF, int ST, int... Ms> DI void k3_step(K3C& c, unsigned trcur, unsigned trnext, unsigned bsoff, unsigned wm0, unsigned wsoff, iseq<Ms...>) { (k3_gap<BUF, ST, Ms>(c, trcur, trnext, bsoff, wm0, wsoff), ...); }
template <int BUF> DI void k3_chunk(K3C& c, unsigned trcur, unsigned trnext, unsigned bsoff, unsigned wm0, unsigned wsoff) {
  k3_step<BUF, 0>(c, trcur, trnext, bsoff, wm0, wsoff, make_iseq<18>{});
  k3_step<BUF, 1>(c, trcur, trnext, bsoff, wm0, wsoff, make_iseq<18>{});
  k3_step<BUF, 2>(c, trcur, trnext, bsoff, wm0, wsoff, make_iseq<18>{});
  asm volatile("s_waitcnt vmcnt(25)\n\ts_barrier" : G_B8((BUF + 1) & 3) :: "memory"); GAP();
  k3_step<BUF, 3>(c, trcur, trnext, bsoff, wm0, wsoff, make_iseq<18>{});
}
template <int... Rs> DI void k3_zero(iseq<Rs...>) { (acc_wr<Rs>(0u), ...); }
template <int I> DI f32x4 k3_acc() { f32x4 r = {acc_rd<4 * I>(), acc_rd<4 * I + 1>(), acc_rd<4 * I + 2>(), acc_rd<4 * I + 3>()}; return r; }
template <int... Is> DI void k3_accs(f32x4 (&a)[18], iseq<Is...>) { ((a[Is] = k3_acc<Is>()), ...); }
template <int BUF, int... Xs> DI void k3_bld_all(K3C& c, unsigned soff, iseq<Xs...>) { (k3_bld<BUF, Xs>(c, soff), ...); }
template <int... Xs> DI void k3_tr_all(K3C& c, unsigned base, iseq<Xs...>) { (k3_tr<0, (Xs >> 1), (Xs & 1)>(c, base), ...); }

__global__ __launch_bounds__(256, 1)
void k3_out(const unsigned short* __restrict__ x, const unsigned short* __restrict__ Wo, const unsigned short* __restrict__ Wd,
            const unsigned short* __restrict__ obuf, const unsigned short* __restrict__ hbuf,
            float* __restrict__ slab, unsigned* __restrict__ flags, unsigned short* __restrict__ out) {
  extern __shared__ __attribute__((aligned(16))) char lds[];
  const unsigned tid = threadIdx.x, lane = tid & 63u;
  const unsigned wid = rfl(tid >> 6);
  const unsigned ldsb = (unsigned)(size_t)lds;
  const unsigned bx = blockIdx.x, xcd = bx & 7u, idx = bx >> 3, role = xcd >> 1, mt = (xcd & 1u) * 8u + (idx >> 2), nt = idx & 3u;
  const bool isF = role >= 2u;
  const unsigned nchunk = isF ? 18u : 16u, steps_row = isF ? 288u : 256u;
  const unsigned k0 = (role & 1u) * nchunk * 128u;
  const unsigned tb = mt * 4u + wid;
  ST_DECL; STR(8); ST(0);
  K3C c;
  const unsigned kg = lane >> 4, q4 = (lane >> 2) & 3u, p4 = lane & 3u;
  c.trb = ldsb + (kg >> 1) * (2u * 9u * 256u) + (4u * (kg & 1u) + q4) * 32u + p4 * 8u;
  #pragma unroll
  for (int p = 0; p < 9; ++p) { const unsigned u = 4u * p + kg, rg = u / 9u, ct = u - 9u * rg; c.wsrc[p] = (8u * rg + ((lane >> 1) & 7u)) * (DM * 2u) + ct * 32u + (lane & 1u) * 16u; }
  #pragma unroll
  for (int tg = 0; tg < 2; ++tg) c.bvo[tg] = (kg >> 1) * 1024u + (kg & 1u) * 512u + (16u * tg + (lane & 15u)) * 16u;
  const unsigned kend = k0 + nchunk * 128u;
  c.srdW = mk_srd(isF ? Wd : Wo, kend * (DM * 2u));
  c.srdA = mk_srd((const char*)(isF ? hbuf : obuf) + (size_t)tb * steps_row * 1024u, (kend >> 4) * 1024u);
  c.m0w = ldsb + wid * 9216u;
  c.sofw = (k0 + wid * 32u) * (DM * 2u) + nt * 288u;
  const unsigned bs0 = (k0 >> 4) * 1024u;
  k3_zero(make_iseq<72>{});
  asm volatile("s_nop 4" ::: "memory");
  #pragma unroll
  for (int p = 0; p < 9; ++p) dma16(c.m0w + 1024u * p, c.wsrc[p], c.srdW, c.sofw);
  k3_bld_all<0>(c, bs0, make_iseq<8>{});
  k3_bld_all<1>(c, bs0 + 8192u, make_iseq<8>{});
  #pragma unroll
  for (int p = 0; p < 9; ++p) dma16(c.m0w + K3SLOT + 1024u * p, c.wsrc[p], c.srdW, c.sofw + 128u * (DM * 2u));
  k3_bld_all<2>(c, bs0 + 16384u, make_iseq<8>{});
  #pragma unroll
  for (int p = 0; p < 9; ++p) dma16(c.m0w + 2u * K3SLOT + 1024u * p, c.wsrc[p], c.srdW, c.sofw + 256u * (DM * 2u));
  asm volatile("s_waitcnt vmcnt(34)\n\ts_barrier" : G_B8(0) :: "memory");
  k3_tr_all(c, c.trb, make_iseq<18>{});
  asm volatile("s_waitcnt lgkmcnt(0)" ::: "memory"); GAP();
  ST(1);
#define K3_CHUNK(BUF, ch) k3_chunk<BUF>(c, c.trb + (BUF) * K3SLOT, c.trb + (((BUF) + 1) & 3) * K3SLOT, bs0 + ((ch) + 3u) * 8192u, c.m0w + (((BUF) + 3) & 3) * K3SLOT, c.sofw + ((ch) + 3u) * 128u * (DM * 2u))
  for (unsigned ch = 0;; ch += 4u) {
    K3_CHUNK(0, ch); K3_CHUNK(1, ch + 1u);
    if (ch + 2u == nchunk) break;
    K3_CHUNK(2, ch + 2u); K3_CHUNK(3, ch + 3u);
    if (ch + 4u == nchunk) break;
  }
  asm volatile("s_waitcnt vmcnt(0) lgkmcnt(0)\n\ts_nop 7\n\ts_nop 7" : G_B8(0), G_B8(1) :: "memory");
  asm volatile("" : G_B8(2), G_B8(3) :: "memory");
  ST(2);
  const unsigned tile = mt * 4u + nt;
  f32x4 acc[18]; k3_accs(acc, make_iseq<18>{});
  u32x2 xr[5];
  const unsigned tok0 = mt * 128u + wid * 32u + (lane & 15u), col0 = nt * 144u + 4u * (lane >> 4);
  #pragma unroll
  for (int n = 0; n < 5; ++n) { const unsigned i = (role + 4u * n < 18u) ? role + 4u * n : role; xr[n] = *(const u32x2*)(x + (size_t)(tok0 + 16u * (i & 1u)) * DM + col0 + 16u * (i >> 1)); }
  const srd_t srdS = mk_srd(slab, 64u * 16u * 10240u);
  const unsigned mybase = ((tile * 4u + role) * 4u + wid) * 10240u;
  #pragma unroll
  for (int r = 0; r < 4; ++r)
    if ((unsigned)r != role) {
      const u32x4 w0 = {cvtpk(acc[r][0], acc[r][1]), cvtpk(acc[r][2], acc[r][3]), cvtpk(acc[r + 4][0], acc[r + 4][1]), cvtpk(acc[r + 4][2], acc[r + 4][3])};
      const u32x4 w1 = {cvtpk(acc[r + 8][0], acc[r + 8][1]), cvtpk(acc[r + 8][2], acc[r + 8][3]), cvtpk(acc[r + 12][0], acc[r + 12][1]), cvtpk(acc[r + 12][2], acc[r + 12][3])};
      __builtin_amdgcn_raw_buffer_store_b128(w0, srdS, lane * 16u, mybase + r * 2560u, 16);
      __builtin_amdgcn_raw_buffer_store_b128(w1, srdS, lane * 16u, mybase + r * 2560u + 1024u, 16);
      if (r < 2) { const u32x2 w2 = {cvtpk(acc[(r & 1) + 16][0], acc[(r & 1) + 16][1]), cvtpk(acc[(r & 1) + 16][2], acc[(r & 1) + 16][3])}; __builtin_amdgcn_raw_buffer_store_b64(w2, srdS, lane * 8u, mybase + r * 2560u + 2048u, 16); }
    }
  asm volatile("s_waitcnt vmcnt(0)" ::: "memory");
  __syncthreads();
  ST(3);
  gu32* fl = (gu32*)flags;
  if (tid == 0) __hip_atomic_store(fl + tile * 4u + role, 1u, __ATOMIC_RELAXED, __HIP_MEMORY_SCOPE_AGENT);
  if (wid == 0) {
    #pragma unroll
    for (unsigned r = 1; r < 4; ++r) {
      gu32* w = fl + tile * 4u + ((role + r) & 3u);
      while ((unsigned)__builtin_amdgcn_readfirstlane(__hip_atomic_load(w, __ATOMIC_RELAXED, __HIP_MEMORY_SCOPE_AGENT)) != 1u) __builtin_amdgcn_s_sleep(1);
    }
  }
  asm volatile("" ::: "memory");
  __syncthreads();
  ST(4);
  u32x4 pa[3], pb[3]; u32x2 pc[3];
  #pragma unroll
  for (unsigned q = 0; q < 3; ++q) {
    const unsigned src = ((tile * 4u + ((role + 1u + q) & 3u)) * 4u + wid) * 10240u + role * 2560u;
    pa[q] = __builtin_amdgcn_raw_buffer_load_b128(srdS, lane * 16u, src, 16);
    pb[q] = __builtin_amdgcn_raw_buffer_load_b128(srdS, lane * 16u, src + 1024u, 16);
    pc[q] = u32x2{0u, 0u};
    if (role < 2u) pc[q] = __builtin_amdgcn_raw_buffer_load_b64(srdS, lane * 8u, src + 2048u, 16);
  }
  #pragma unroll
  for (int n = 0; n < 5; ++n) {
    const unsigned i = role + 4u * n;
    if (i < 18u) {
      f32x4 own = acc[0];
      #pragma unroll
      for (int ii = 0; ii < 18; ++ii) if ((ii >> 2) == n && (unsigned)(ii & 3) == role) own = acc[ii];
      f32x4 pq[3];
      #pragma unroll
      for (int q = 0; q < 3; ++q) {
        const unsigned w0 = n == 4 ? pc[q][0] : (n < 2 ? pa[q][2 * (n & 1)] : pb[q][2 * (n & 1)]), w1 = n == 4 ? pc[q][1] : (n < 2 ? pa[q][2 * (n & 1) + 1] : pb[q][2 * (n & 1) + 1]);
        pq[q] = f32x4{__uint_as_float(w0 << 16), __uint_as_float(w0 & 0xffff0000u), __uint_as_float(w1 << 16), __uint_as_float(w1 & 0xffff0000u)};
      }
      const bool odd = role & 1u;
      float o[4];
      #pragma unroll
      for (int e = 0; e < 4; ++e) {
        const float mine = own[e] + (odd ? pq[2][e] : pq[0][e]), other = pq[1][e] + (odd ? pq[0][e] : pq[2][e]);
        const float s = isF ? other : mine, f = isF ? mine : other;
        const float g = f * __builtin_amdgcn_rcpf(1.f + __builtin_amdgcn_exp2f(-1.4426950408889634f * f));
        const float xv = __uint_as_float((e & 1) ? (xr[n][e >> 1] & 0xffff0000u) : (xr[n][e >> 1] << 16));
        o[e] = xv + s * g;
      }
      u32x2 w = {cvtpk(o[0], o[1]), cvtpk(o[2], o[3])};
      *(u32x2*)(out + (size_t)(tok0 + 16u * (i & 1u)) * DM + col0 + 16u * (i >> 1)) = w;
    }
  }
  ST(5); STR(9);
  ST_OUT((char*)flags + (10u << 20));
}

extern "C" void kernel_launch(void* const* d_in, const int* in_sizes, int n_in, void* d_out, int out_size, void* d_ws, size_t ws_size, hipStream_t stream) {
  const unsigned short* x  = (const unsigned short*)d_in[0];
  const unsigned short* kc = (const unsigned short*)d_in[1];
  const unsigned short* vc = (const unsigned short*)d_in[2];
  const unsigned short* wq = (const unsigned short*)d_in[3];
  const unsigned short* wo = (const unsigned short*)d_in[4];
  const unsigned short* wg = (const unsigned short*)d_in[6];
  const unsigned short* wu = (const unsigned short*)d_in[7];
  const unsigned short* wd = (const unsigned short*)d_in[8];
  char* ws = (char*)d_ws;
  (void)hipFuncSetAttribute((const void*)k2_main, hipFuncAttributeMaxDynamicSharedMemorySize, (int)SHM_K2);
  (void)hipFuncSetAttribute((const void*)k3_out, hipFuncAttributeMaxDynamicSharedMemorySize, (int)SHM_K3);
  k1_layernorm<<<256, 256, 0, stream>>>(x, (unsigned short*)(ws + WS_XN), (unsigned*)(ws + WS_FLAG), wg, wu, wq);
  k2_main<<<256, 256, SHM_K2, stream>>>(kc, vc, wq, wg, wu, (const unsigned short*)(ws + WS_XN), (unsigned short*)(ws + WS_H), (unsigned short*)(ws + WS_O), wo, wd);
  k3_out<<<256, 256, SHM_K3, stream>>>(x, wo, wd, (const unsigned short*)(ws + WS_O), (const unsigned short*)(ws + WS_H), (float*)(ws + WS_SLAB), (unsigned*)(ws + WS_FLAG), (unsigned short*)d_out);
}
```

```cpp
#include <hip/hip_runtime.h>
#include <hip/hip_bf16.h>
#include <utility>

typedef float    f32x16 __attribute__((ext_vector_type(16)));
typedef float    f32x2  __attribute__((ext_vector_type(2)));
typedef float    f32x4  __attribute__((ext_vector_type(4)));
typedef __bf16   bf16x8 __attribute__((ext_vector_type(8)));
typedef __bf16   bf16x4 __attribute__((ext_vector_type(4)));
typedef __bf16   bf16x2 __attribute__((ext_vector_type(2)));
typedef unsigned u32x4  __attribute__((ext_vector_type(4)));
typedef unsigned u32x2  __attribute__((ext_vector_type(2)));
template <int... Is> using iseq = std::integer_sequence<int, Is...>;
template <int N> using make_iseq = std::make_integer_sequence<int, N>;
typedef __amdgpu_buffer_rsrc_t srd_t;

constexpr int SEQ = 2048, DM = 576, NH = 32, DH = 128, FF = 4608;
constexpr size_t WS_XN = 0;
constexpr size_t WS_H = 4u << 20;
constexpr size_t WS_O = 24u << 20;
constexpr size_t WS_SLAB = 48u << 20;
constexpr size_t WS_FLAG = 72u << 20;

#define ST_DECL
#define ST(i)
#define STR(i)
#define ST_OUT(base)
#define SRD_WORD3 0x00020000
#define DI __device__ __forceinline__
#define GAP() __builtin_amdgcn_sched_barrier(0)
#define ACLOB_LO "a0","a1","a2","a3","a4","a5","a6","a7","a8","a9","a10","a11","a12","a13","a14","a15","a16","a17","a18","a19","a20","a21","a22","a23","a24","a25","a26","a27","a28","a29","a30","a31","a32","a33","a34","a35","a36","a37","a38","a39","a40","a41","a42","a43","a44","a45","a46","a47","a48","a49","a50","a51","a52","a53","a54","a55","a56","a57","a58","a59","a60","a61","a62","a63","a64","a65","a66","a67","a68","a69","a70","a71","a72","a73","a74","a75","a76","a77","a78","a79","a80","a81","a82","a83","a84","a85","a86","a87","a88","a89","a90","a91","a92","a93","a94","a95","a96","a97","a98","a99","a100","a101","a102","a103","a104","a105","a106","a107","a108","a109","a110","a111","a112","a113","a114","a115","a116","a117","a118","a119","a120","a121","a122","a123","a124","a125","a126","a127"
#define ACLOB_HI "a128","a129","a130","a131","a132","a133","a134","a135","a136","a137","a138","a139","a140","a141","a142","a143","a144","a145","a146","a147","a148","a149","a150","a151","a152","a153","a154","a155","a156","a157","a158","a159","a160","a161","a162","a163","a164","a165","a166","a167","a168","a169","a170","a171","a172","a173","a174","a175","a176","a177","a178","a179","a180","a181","a182","a183","a184","a185","a186","a187","a188","a189","a190","a191","a192","a193","a194","a195","a196","a197","a198","a199","a200","a201","a202","a203","a204","a205","a206","a207","a208","a209","a210","a211","a212","a213","a214","a215","a216","a217","a218","a219","a220","a221","a222","a223","a224","a225","a226","a227","a228","a229","a230","a231","a232","a233","a234","a235","a236","a237","a238","a239","a240","a241","a242","a243","a244","a245","a246","a247","a248","a249","a250","a251","a252","a253","a254","a255"

DI unsigned rfl(unsigned x) { return __builtin_amdgcn_readfirstlane(x); }
DI unsigned cvtpk(float lo, float hi) { f32x2 v = {lo, hi}; bf16x2 b = __builtin_convertvector(v, bf16x2); return __builtin_bit_cast(unsigned, b); }
DI float max3f(float a, float b, float c) { float r; asm("v_max3_f32 %0, %1, %2, %3" : "=v"(r) : "v"(a), "v"(b), "v"(c)); return r; }
DI float max2f(float a, float b) { float r; asm("v_max_f32_e32 %0, %1, %2" : "=v"(r) : "v"(a), "v"(b)); return r; }
DI float fadd_s(float a, float b) { float r; asm("v_add_f32_e32 %0, %1, %2" : "=v"(r) : "v"(a), "v"(b)); return r; }
DI float fadd_v(float a, float b) { float r; asm volatile("v_add_f32_e32 %0, %1, %2" : "=v"(r) : "v"(a), "v"(b)); return r; }
DI float max3v(float a, float b, float c) { float r; asm volatile("v_max3_f32 %0, %1, %2, %3" : "=v"(r) : "v"(a), "v"(b), "v"(c)); return r; }
DI float max2v(float a, float b) { float r; asm volatile("v_max_f32_e32 %0, %1, %2" : "=v"(r) : "v"(a), "v"(b)); return r; }
DI float fmul_s(float a, float b) { float r; asm("v_mul_f32_e32 %0, %1, %2" : "=v"(r) : "v"(a), "v"(b)); return r; }
DI float fsub_s(float a, float b) { float r; asm("v_sub_f32_e32 %0, %1, %2" : "=v"(r) : "v"(a), "v"(b)); return r; }
DI float ffma_s(float a, float b, float c) { float r; asm("v_fma_f32 %0, %1, %2, %3" : "=v"(r) : "v"(a), "v"(b), "v"(c)); return r; }
DI srd_t mk_srd(const void* p, unsigned bytes) { return __builtin_amdgcn_make_buffer_rsrc((void*)p, (short)0, (int)bytes, SRD_WORD3); }
template <int R> DI float acc_rd() { float r; asm volatile("v_accvgpr_read_b32 %0, a[%c1]" : "=v"(r) : "i"(R)); return r; }
template <int R> DI void acc_wr(unsigned w) { asm volatile("v_accvgpr_write_b32 a[%c1], %0" :: "v"(w), "i"(R) : ACLOB_LO, ACLOB_HI); }
template <int N> DI void wait_vm() { asm volatile("s_waitcnt vmcnt(%c0)" :: "i"(N) : "memory"); }
DI void dma16(unsigned m0v, unsigned voff, srd_t srd, unsigned soff) {
  asm volatile("s_mov_b32 m0, %0\n\ts_nop 0\n\tbuffer_load_dwordx4 %1, %2, %3 offen lds" :: "s"(m0v), "v"(voff), "s"(srd), "s"(soff) : "m0", "memory");
}

DI void set_m0(unsigned m0v) { asm volatile("s_mov_b32 m0, %0" :: "s"(m0v) : "m0"); }
DI void dma16_m0(unsigned voff, srd_t srd, unsigned soff) {
  asm volatile("buffer_load_dwordx4 %0, %1, %2 offen lds" :: "v"(voff), "s"(srd), "s"(soff) : "memory");
}
#define XN_AUX 16
__global__ __launch_bounds__(256) void k1_layernorm(const unsigned short* __restrict__ x, unsigned short* __restrict__ xn, unsigned* __restrict__ flags) {
  __shared__ __attribute__((aligned(16))) unsigned long long stg[72 * 16];
  const unsigned tid = threadIdx.x, l = tid & 31u, tok = tid >> 5;
  ST_DECL; STR(8);
  if (blockIdx.x == 0) flags[tid] = 0u;
  const unsigned t = blockIdx.x * 8u + tok;
  const u32x4* row = (const u32x4*)(x + (size_t)t * DM);
  u32x4 v[3];
  v[0] = row[l]; v[1] = row[l + 32]; v[2] = (l < 8u) ? row[l + 64] : (u32x4){0u, 0u, 0u, 0u};
  float f[3][8]; float s = 0.f;
  #pragma unroll
  for (int c = 0; c < 3; ++c)
    #pragma unroll
    for (int e = 0; e < 4; ++e) { f[c][2 * e] = __uint_as_float(v[c][e] << 16); f[c][2 * e + 1] = __uint_as_float(v[c][e] & 0xffff0000u); s += f[c][2 * e] + f[c][2 * e + 1]; }
  #pragma unroll
  for (int m = 16; m >= 1; m >>= 1) s += __shfl_xor(s, m, 32);
  const float mean = s * (1.f / DM);
  float q = 0.f;
  #pragma unroll
  for (int c = 0; c < 3; ++c)
    #pragma unroll
    for (int e = 0; e < 8; ++e) { const float d = f[c][e] - mean; f[c][e] = d; if (c < 2 || l < 8u) q += d * d; }
  #pragma unroll
  for (int m = 16; m >= 1; m >>= 1) q += __shfl_xor(q, m, 32);
  const float rstd = 1.f / sqrtf(q * (1.f / DM) + 1e-5f);
  #pragma unroll
  for (int c = 0; c < 3; ++c) {
    if (c < 2 || l < 8u) {
      const unsigned m = l + 32u * c, sstep = m >> 1, jj = m & 1u;
      const unsigned long long lo = (unsigned long long)cvtpk(f[c][0] * rstd, f[c][1] * rstd) | ((unsigned long long)cvtpk(f[c][2] * rstd, f[c][3] * rstd) << 32);
      const unsigned long long hi = (unsigned long long)cvtpk(f[c][4] * rstd, f[c][5] * rstd) | ((unsigned long long)cvtpk(f[c][6] * rstd, f[c][7] * rstd) << 32);
      stg[(2u * sstep) * 16u + tok * 2u + jj] = lo;
      stg[(2u * sstep + 1u) * 16u + tok * 2u + jj] = hi;
    }
  }
  __syncthreads();
  const unsigned tb = blockIdx.x >> 2, tbase = (blockIdx.x & 3u) * 8u;
  for (unsigned u = tid; u < 576u; u += 256u) {
    const unsigned line = u >> 3, tk = u & 7u;
    const u32x4 w = *(const u32x4*)&stg[line * 16u + tk * 2u];
    __builtin_amdgcn_raw_buffer_store_b128(w, mk_srd(xn, 64u * 36u * 1024u), (tb * 36u + (line >> 1)) * 1024u + (line & 1u) * 512u + (tbase + tk) * 16u, 0, XN_AUX);
  }
  STR(7);
  STR(9);
  ST_OUT((char*)flags + (12u << 20));
}

#define KRING 0x0000u
#define VRING 0x8000u
#define WRING 0x10000u
constexpr unsigned WSLOT = 20480u;
constexpr size_t SHM_K2 = 0x10000 + 3 * 20480;
constexpr unsigned KRS = 256u, KTILEB = 64u * KRS;
constexpr int NT_KV = SEQ / 64;
constexpr float THR = 8.0f;

constexpr int g_nt(int gc) { return gc / 9 == 1 ? 5 : 4; }
constexpr int g_ngaps(int gc) { return 8 * g_nt(gc); }
constexpr int EPI_OPS = 42;
constexpr int epi_units(int ph) { return ph == 1 ? 16 : (ph == 2 ? 20 : 0); }
constexpr int epi_lo(int ph, int G) { const int tg = 9 * 8 * (ph == 1 ? 5 : 4), to = epi_units(ph) * EPI_OPS; return G >= tg ? to : (int)(((long)G * to + tg - 1) / tg); }
constexpr int epi_stores(int gc, int M) {
  const int ph = gc / 9; if (ph == 0) return 0;
  const int G = (gc % 9) * g_ngaps(gc) + M; int n = 0;
  for (int o = epi_lo(ph, G); o < epi_lo(ph, G + 1); ++o) if (o % EPI_OPS == EPI_OPS - 1 && (o / EPI_OPS) % 2 == 1) ++n;
  return n;
}
#define H_AUX 16
#define QPF_CHUNK 9
constexpr int g_npiece(int gc) { return gc > 26 ? 0 : (gc / 9 == 1 ? 5 : 4); }
constexpr int g_xload(int gc, int M) { const int nt = g_nt(gc), ks = M / (2 * nt), i = M % (2 * nt); return (gc + 1 <= 26 && ks < 2 && i >= nt && i < nt + 4) ? 4 * ks + (i - nt) : -1; }
constexpr int g_wpiece(int gc, int M) { const int nt = g_nt(gc), ks = M / (2 * nt), i = M % (2 * nt), p = g_npiece(gc + 2); return (p > 0 && ks == 2 && i >= 2 * nt - p) ? i - (2 * nt - p) : -1; }
constexpr int g_kvpiece(int gc, int M) {
  const int ks = M / 8, i = M % 8;
  if (gc == 25 && ks == 2 && i >= 4) return i - 4;
  if (gc == 26 && ks < 2 && i >= 4) return 4 + 4 * ks + (i - 4);
  return -1;
}
constexpr int g_qpf(int gc, int M) { return (gc == QPF_CHUNK && M >= 30 && M < 35) ? M - 30 : -1; }
constexpr int g_nvm(int gc, int M) { return (g_qpf(gc, M) >= 0) + (g_xload(gc, M) >= 0) + (g_wpiece(gc, M) >= 0) + (g_kvpiece(gc, M) >= 0) + epi_stores(gc, M); }
constexpr int g_vm_range(int gc, int m0, int m1) { int n = 0; for (int m = m0; m < m1; ++m) n += g_nvm(gc, m); return n; }
constexpr int g_wait_bar(int gc) { const int nt = g_nt(gc); return g_vm_range(gc, 0, 6 * nt) + (gc > 0 ? g_vm_range(gc - 1, 6 * g_nt(gc - 1), 8 * g_nt(gc - 1)) : 0); }
constexpr int g_wait_end(int gc) { const int nt = g_nt(gc); return g_vm_range(gc, 2 * nt + nt + 4, 8 * nt); }

struct GC {
  unsigned trb[3];
  unsigned wsrc1, wsrc2[5], wsrc3, xvo[2], hvo;
  srd_t srdX, srdGU, srdWq, srdH, srdK, srdV;
  unsigned m0g1, m0g2, m0q, sof1, sof2, sofq, hso;
  unsigned vksrc[2], vvsrc, m0k, m0v, sofk, sofv, pfq, pfm;
  bf16x8 B[2][4][2];
  bf16x4 alo[2][5], aup[2][5];
  bf16x8 A[2][5];
  f32x16 accV[2];
  float eg[4], eu[4], et[4], egu[4], kc0, kc1; u32x4 hw;
};

template <int PH, int T, int R> DI float epi_src(GC& c) {
  if constexpr (PH == 1) return acc_rd<16 * T + R>();
  else if constexpr (T < 8) return acc_rd<128 + 16 * T + R>();
  else return c.accV[T - 8][R];
}
template <int PH, int O> DI void epi_op(GC& c) {
  constexpr int u = O / EPI_OPS, o = O % EPI_OPS, T = u >> 1, pp = u & 1, i = o & 3;
  if constexpr (o < 4)                 c.eg[i] = epi_src<PH, T, 4 * pp + i>(c);
  else if constexpr (o < 8)            c.eu[i] = epi_src<PH, T, 8 + 4 * pp + i>(c);
  else if constexpr (o < 12)           asm volatile("v_mul_f32_e32 %0, %1, %1" : "=v"(c.et[i]) : "v"(c.eg[i]));
  else if constexpr (o < 16)           asm volatile("v_fma_f32 %0, %0, %1, %2" : "+v"(c.et[i]) : "v"(c.kc1), "v"(c.kc0));
  else if constexpr (o < 20)           asm volatile("v_mul_f32_e32 %0, %0, %1" : "+v"(c.et[i]) : "v"(c.eg[i]));
  else if constexpr (o < 28) {
    constexpr int k = (o - 20) >> 1;
    if constexpr (((o - 20) & 1) == 0) asm volatile("v_exp_f32_e32 %0, %0" : "+v"(c.et[k]));
    else                               asm volatile("v_mul_f32_e32 %0, %1, %2" : "=v"(c.egu[k]) : "v"(c.eg[k]), "v"(c.eu[k]));
  }
  else if constexpr (o < 32)           asm volatile("v_add_f32_e32 %0, 1.0, %0" : "+v"(c.et[i]));
  else if constexpr (o < 36)           asm volatile("v_rcp_f32_e32 %0, %0" : "+v"(c.et[i]));
  else if constexpr (o < 40)           asm volatile("v_mul_f32_e32 %0, %0, %1" : "+v"(c.egu[i]) : "v"(c.et[i]));
  else {
    constexpr int k = o - 40;
    unsigned w; asm volatile("v_cvt_pk_bf16_f32 %0, %1, %2" : "=v"(w) : "v"(c.egu[2 * k]), "v"(c.egu[2 * k + 1]));
    c.hw[2 * pp + k] = w;
    if constexpr (k == 1 && pp == 1) {
      constexpr int ctg = (PH == 1 ? 0 : 4) + (T >> 1), qb = T & 1;
      __builtin_amdgcn_raw_buffer_store_b128(c.hw, c.srdH, c.hvo, c.hso + (unsigned)(qb * 288 + ctg) * 1024u, H_AUX);
    }
  }
}
template <int PH, int O0, int... Is> DI void epi_ops(GC& c, iseq<Is...>) { (epi_op<PH, O0 + Is>(c), ...); }

template <int GCI, int KS, int CT, int HALF> DI void g_trread(GC& c) {
  constexpr int ph = GCI / 9, nt = g_nt(GCI), slot = GCI % 3, par = KS & 1;
  constexpr int off = slot * (int)WSLOT + (ph == 2 ? 4096 * KS + 2048 * HALF + 512 * CT : ((2 * KS + HALF) * nt + CT) * 256);
  if constexpr (HALF == 0) asm volatile("ds_read_b64_tr_b16 %0, %1 offset:%c2" : "=v"(c.alo[par][CT]) : "v"(c.trb[ph]), "i"(off) : "memory");
  else                     asm volatile("ds_read_b64_tr_b16 %0, %1 offset:%c2" : "=v"(c.aup[par][CT]) : "v"(c.trb[ph]), "i"(off) : "memory");
}
template <int GCI, int X> DI void g_xld(GC& c) {
  constexpr int ks = X >> 1, qb = X & 1;
  asm volatile("buffer_load_dwordx4 %0, %1, %2, %3 offen offset:%c4" : "=v"(c.B[GCI & 1][ks][qb]) : "v"(c.xvo[qb]), "s"(c.srdX), "s"((unsigned)((GCI % 9) * 4096)), "i"(ks * 1024) : "memory");
}
template <int GCI, int P> DI void g_wdma(GC& c, unsigned ldsw) {
  constexpr int ph = GCI / 9, cc = GCI % 9; constexpr unsigned slot = (GCI % 3) * WSLOT;
  if constexpr (ph == 0)      dma16(c.m0g1 + slot + 1024u * P, c.wsrc1, c.srdGU, c.sof1 + (unsigned)(cc * 64 + 8 * P) * (FF * 2u));
  else if constexpr (ph == 1) dma16(c.m0g2 + slot + 1024u * P, c.wsrc2[P], c.srdGU, c.sof2 + (unsigned)(cc * 64) * (FF * 2u));
  else                        dma16(c.m0q + slot + 1024u * P, c.wsrc3, c.srdWq, c.sofq + (unsigned)(cc * 64 + 8 * (P >> 1)) * 8192u + 128u * (P & 1));
}
template <int P> DI void g_kvdma(GC& c) {
  constexpr int q = P & 3;
  if constexpr (P < 4)      dma16(c.m0k + 1024u * q, c.vksrc[q & 1], c.srdK, c.sofk + KRS * 4u * q);
  else if constexpr (P < 8) dma16(c.m0v + 1024u * q, c.vvsrc, c.srdV, c.sofv + KRS * 8u * (q >> 1) + 128u * (q & 1));
  else                      dma16(c.m0k + 0x4000u + 1024u * q, c.vksrc[q & 1], c.srdK, KTILEB + c.sofk + KRS * 4u * q);
}
template <int GCI, int M> DI void g_gap(GC& c) {
  constexpr int ph = GCI / 9, cc = GCI % 9, nt = g_nt(GCI), ks = M / (2 * nt), i = M % (2 * nt), ct = i >> 1, qb = i & 1;
  constexpr bool Z = (cc == 0 && ks == 0);
  if constexpr (ph == 1 && ct == 4) {
    if constexpr (Z) asm volatile("v_mfma_f32_32x32x16_bf16 %0, %1, %2, 0" : "=&v"(c.accV[qb]) : "v"(c.A[ks & 1][ct]), "v"(c.B[GCI & 1][ks][qb]));
    else             asm volatile("v_mfma_f32_32x32x16_bf16 %0, %1, %2, %0" : "+v"(c.accV[qb]) : "v"(c.A[ks & 1][ct]), "v"(c.B[GCI & 1][ks][qb]));
  } else {
    constexpr int ao = (ph == 1 ? 128 : 0) + 16 * (2 * ct + qb);
    if constexpr (Z) asm volatile("v_mfma_f32_32x32x16_bf16 a[%c2:%c3], %0, %1, 0" :: "v"(c.A[ks & 1][ct]), "v"(c.B[GCI & 1][ks][qb]), "i"(ao), "i"(ao + 15) : ACLOB_LO, ACLOB_HI);
    else             asm volatile("v_mfma_f32_32x32x16_bf16 a[%c2:%c3], %0, %1, a[%c2:%c3]" :: "v"(c.A[ks & 1][ct]), "v"(c.B[GCI & 1][ks][qb]), "i"(ao), "i"(ao + 15) : ACLOB_LO, ACLOB_HI);
  }
  constexpr int ngc = ks == 3 ? GCI + 1 : GCI, nks = (ks + 1) & 3;
  if constexpr (ngc <= 26 && i < g_nt(ngc)) { g_trread<ngc, nks, i, 0>(c); g_trread<ngc, nks, i, 1>(c); }
  if constexpr (g_xload(GCI, M) >= 0) g_xld<GCI + 1, g_xload(GCI, M)>(c);
  if constexpr (g_wpiece(GCI, M) >= 0) g_wdma<GCI + 2, g_wpiece(GCI, M)>(c, 0u);
  if constexpr (g_kvpiece(GCI, M) >= 0) g_kvdma<g_kvpiece(GCI, M)>(c);
  if constexpr (g_qpf(GCI, M) >= 0) dma16(c.pfm, c.hvo, c.srdWq, c.pfq + 1024u * g_qpf(GCI, M));
  if constexpr (ph > 0) { constexpr int G = cc * g_ngaps(GCI) + M, o0 = epi_lo(ph, G), o1 = epi_lo(ph, G + 1); epi_ops<ph, o0>(c, make_iseq<o1 - o0>{}); }
  GAP();
}
#define G_A5(p) "+v"(c.alo[p][0]), "+v"(c.alo[p][1]), "+v"(c.alo[p][2]), "+v"(c.alo[p][3]), "+v"(c.alo[p][4]), "+v"(c.aup[p][0]), "+v"(c.aup[p][1]), "+v"(c.aup[p][2]), "+v"(c.aup[p][3]), "+v"(c.aup[p][4])
#define G_A4(p) "+v"(c.alo[p][0]), "+v"(c.alo[p][1]), "+v"(c.alo[p][2]), "+v"(c.alo[p][3]), "+v"(c.aup[p][0]), "+v"(c.aup[p][1]), "+v"(c.aup[p][2]), "+v"(c.aup[p][3])
#define G_B8(b) "+v"(c.B[b][0][0]), "+v"(c.B[b][0][1]), "+v"(c.B[b][1][0]), "+v"(c.B[b][1][1]), "+v"(c.B[b][2][0]), "+v"(c.B[b][2][1]), "+v"(c.B[b][3][0]), "+v"(c.B[b][3][1])
template <int NTN, int PAR> DI void g_land(GC& c) {
  if constexpr (NTN == 5) asm volatile("s_waitcnt lgkmcnt(0)" : G_A5(PAR) :: "memory"); else asm volatile("s_waitcnt lgkmcnt(0)" : G_A4(PAR) :: "memory");
  GAP();
  #pragma unroll
  for (int ct = 0; ct < NTN; ++ct) c.A[PAR][ct] = __builtin_shufflevector(c.alo[PAR][ct], c.aup[PAR][ct], 0, 1, 2, 3, 4, 5, 6, 7);
  if constexpr (NTN == 5) asm volatile("s_nop 1" : "+v"(c.A[PAR][0]), "+v"(c.A[PAR][1]), "+v"(c.A[PAR][2]), "+v"(c.A[PAR][3]), "+v"(c.A[PAR][4]));
  else                    asm volatile("s_nop 1" : "+v"(c.A[PAR][0]), "+v"(c.A[PAR][1]), "+v"(c.A[PAR][2]), "+v"(c.A[PAR][3]));
  GAP();
}
template <int GCI, int M0, int... Is> DI void g_gaps(GC& c, iseq<Is...>) { (g_gap<GCI, M0 + Is>(c), ...); }
template <int GCI> DI void g_chunk(GC& c) {
  constexpr int nt = g_nt(GCI), n2 = 2 * nt;
  g_gaps<GCI, 0>(c, make_iseq<n2>{});       g_land<nt, 1>(c);
  g_gaps<GCI, n2>(c, make_iseq<n2>{});      g_land<nt, 0>(c);
  g_gaps<GCI, 2 * n2>(c, make_iseq<n2>{});  g_land<nt, 1>(c);
  if constexpr (GCI < 26) { wait_vm<g_wait_bar(GCI)>(); asm volatile("s_barrier" ::: "memory"); GAP(); }
  g_gaps<GCI, 3 * n2>(c, make_iseq<n2>{});
  if constexpr (GCI < 26) {
    asm volatile("s_waitcnt vmcnt(%c8)" : G_B8((GCI + 1) & 1) : "i"(g_wait_end(GCI)) : "memory");
    g_land<g_nt(GCI + 1), 0>(c);
  }
}
template <int G0, int... Gs> DI void g_chunks(GC& c, iseq<Gs...>) { (g_chunk<G0 + Gs>(c), ...); }
template <int... Ps> DI void g_wdma_all0(GC& c, iseq<Ps...>) { (g_wdma<0, Ps>(c, 0u), ...); }
template <int... Ps> DI void g_wdma_all1(GC& c, iseq<Ps...>) { (g_wdma<1, Ps>(c, 0u), ...); }
template <int... Xs> DI void g_xld_all0(GC& c, iseq<Xs...>) { (g_xld<0, Xs>(c), ...); }
template <int... Xs> DI void g_tr_all0(GC& c, iseq<Xs...>) { (g_trread<0, 0, (Xs >> 1), (Xs & 1)>(c), ...); }
template <int I> DI void q_pack(float sc) {
  constexpr int qb = I >> 5, d0 = (I >> 2) & 7, e = I & 3, src = 16 * (2 * (d0 >> 1) + qb) + 8 * (d0 & 1) + 2 * e;
  const float a = acc_rd<src>(), b = acc_rd<src + 1>();
  acc_wr<128 + 32 * qb + 4 * d0 + e>(cvtpk(a * sc, b * sc));
}
template <int... Is> DI void q_packs(float sc, iseq<Is...>) { (q_pack<Is>(sc), ...); }

DI float w4_aone() { unsigned l; asm volatile("v_mbcnt_lo_u32_b32 %0, -1, 0\n\tv_mbcnt_hi_u32_b32 %0, -1, %0" : "=v"(l)); return l < 32u ? 1.f : 0.f; }
DI void negm_bcast(f32x16& negm, float aone, float neg_mhat) { asm volatile("s_nop 1\n\tv_mfma_f32_32x32x2_f32 %0, %1, %2, 0" : "+v"(negm) : "v"(aone), "v"(neg_mhat)); }
struct W4C {
  unsigned vkb[4], vvb, vksrc[2], vvsrc, m0k, m0v, sofk, sofv;
  srd_t srdK, srdV, srdP; unsigned pfm0, pfvo, pfso;
  f32x16 S[2][2][2];
  f32x16 NEGM[2];
  bf16x8 Vf[4][4], Pk[2][4];
  bf16x4 vlo[4][4], vup[4][4];
  float mhat[2], lsum[2], fct[2]; bool pend;
  float rs0[2], rs1[2], pmx[2][4], rmax[2], delta[2];
  unsigned pm0[8], psf[8];
};
constexpr int AO_(int db, int qb) { return 16 * (2 * db + qb); }
constexpr int AQ_(int qb, int d0) { return 128 + 32 * qb + 4 * d0; }
constexpr int AK_(int kb, int d0) { return 192 + 32 * kb + 4 * d0; }
template <int I> DI void tr_read(W4C& c, unsigned vslot) {
  constexpr int ks = I >> 3, db = (I >> 1) & 3;
  if constexpr ((I & 1) == 0) asm volatile("ds_read_b64_tr_b16 %0, %1 offset:%2" : "=v"(c.vlo[db][ks]) : "v"(c.vvb + vslot), "i"(4096 * ks + 512 * db) : "memory");
  else                        asm volatile("ds_read_b64_tr_b16 %0, %1 offset:%2" : "=v"(c.vup[db][ks]) : "v"(c.vvb + vslot), "i"(4096 * ks + 2048 + 512 * db) : "memory");
}
template <int I> DI void k_read(W4C& c, unsigned slot) {
  constexpr int kb = I >> 3, d0 = I & 7;
  asm volatile("ds_read_b128 a[%c1:%c2], %0 offset:%c3" :: "v"(c.vkb[d0 & 3] + slot), "i"(AK_(kb, d0)), "i"(AK_(kb, d0) + 3), "i"(8192 * kb + 128 * (d0 >> 2)) : "memory", ACLOB_HI);
}
template <int X, bool FIRST, int M> DI void qk_mfma(W4C& c) {
  constexpr int d0 = M >> 2, kb = (M >> 1) & 1, qb = M & 1, ak = AK_(kb, d0), aq = AQ_(qb, d0);
  if constexpr (d0 == 0) {
    if constexpr (FIRST) asm volatile("v_mfma_f32_32x32x16_bf16 %0, a[%c1:%c2], a[%c3:%c4], 0" : "=&v"(c.S[X][kb][qb]) : "i"(ak), "i"(ak + 3), "i"(aq), "i"(aq + 3) : ACLOB_LO, ACLOB_HI);
    else                 asm volatile("v_mfma_f32_32x32x16_bf16 %0, a[%c2:%c3], a[%c4:%c5], %1" : "=&v"(c.S[X][kb][qb]) : "v"(c.NEGM[qb]), "i"(ak), "i"(ak + 3), "i"(aq), "i"(aq + 3) : ACLOB_LO, ACLOB_HI);
  } else asm volatile("v_mfma_f32_32x32x16_bf16 %0, a[%c1:%c2], a[%c3:%c4], %0" : "+v"(c.S[X][kb][qb]) : "i"(ak), "i"(ak + 3), "i"(aq), "i"(aq + 3) : ACLOB_LO, ACLOB_HI);
}
template <int M, bool ZERO> DI void pv_mfma(W4C& c) {
  constexpr int ks = M >> 3, db = (M >> 1) & 3, qb = M & 1, ao = AO_(db, qb);
  if constexpr (ZERO && ks == 0) asm volatile("v_mfma_f32_32x32x16_bf16 a[%c2:%c3], %0, %1, 0" :: "v"(c.Vf[db][ks]), "v"(c.Pk[qb][ks]), "i"(ao), "i"(ao + 15) : ACLOB_LO);
  else                           asm volatile("v_mfma_f32_32x32x16_bf16 a[%c2:%c3], %0, %1, a[%c2:%c3]" :: "v"(c.Vf[db][ks]), "v"(c.Pk[qb][ks]), "i"(ao), "i"(ao + 15) : ACLOB_LO);
}
template <int X, int QB, int KS, int P> DI void pack1(W4C& c) {
  u32x4 w = __builtin_bit_cast(u32x4, c.Pk[QB][KS]);
  w[P] = cvtpk(c.S[X][KS >> 1][QB][8 * (KS & 1) + 2 * P], c.S[X][KS >> 1][QB][8 * (KS & 1) + 2 * P + 1]);
  if constexpr (P == 3) asm volatile("" : "+v"(w));
  c.Pk[QB][KS] = __builtin_bit_cast(bf16x8, w);
}
template <int X, int J> DI void rowsum1(W4C& c) {
  constexpr int qb = J >> 4, ks = (J >> 2) & 3, p = J & 3;
  c.rs0[qb] = fadd_v(c.rs0[qb], c.S[X][ks >> 1][qb][8 * (ks & 1) + 2 * p]); c.rs1[qb] = fadd_v(c.rs1[qb], c.S[X][ks >> 1][qb][8 * (ks & 1) + 2 * p + 1]);
}
template <int X, bool FIRST, int M> DI void gapA(W4C& c, unsigned vslot, const int t) {
  constexpr int Xp = X ^ 1;
  qk_mfma<X, FIRST, M>(c);
  GAP();
  if constexpr (M >= 16 && M <= 31) {
    constexpr int q = (M - 16) >> 1;
    if constexpr (((M - 16) & 1) == 0) {
      if constexpr (q < 4) c.pm0[q] = c.m0k + (unsigned)((t + 2) & 1) * 0x4000u + 1024u * q; else c.pm0[q] = c.m0v + (unsigned)((t + 1) & 1) * 0x4000u + 1024u * (q - 4);
      asm volatile("" : "+s"(c.pm0[q]));
    } else {
      if constexpr (q < 4) c.psf[q] = KTILEB * (unsigned)(t + 2) + c.sofk + KRS * 4u * q; else c.psf[q] = KTILEB * (unsigned)(t + 1) + c.sofv + KRS * 8u * ((q - 4) >> 1) + 128u * ((q - 4) & 1);
      asm volatile("" : "+s"(c.psf[q]));
    }
  }
  if constexpr (!FIRST && M == 8) asm volatile("" :: "v"(c.NEGM[0]), "v"(c.NEGM[1]));
  if constexpr (!FIRST) {
    constexpr int base = M + (M >= 4) + (M >= 8) + (M >= 12) + (M >= 16) + (M >= 20) + (M >= 24);
    if constexpr (M <= 25) tr_read<base>(c, vslot);
    if constexpr (M == 3 || M == 7 || M == 11 || M == 15 || M == 19 || M == 23) tr_read<base + 1>(c, vslot);
    if constexpr (M < 16) {
      constexpr int e0 = 2 * M, e1 = 2 * M + 1;
      c.S[Xp][1][e0 >> 4][e0 & 15] = __builtin_amdgcn_exp2f(c.S[Xp][1][e0 >> 4][e0 & 15]);
      c.S[Xp][1][e1 >> 4][e1 & 15] = __builtin_amdgcn_exp2f(c.S[Xp][1][e1 >> 4][e1 & 15]);
      pack1<Xp, (M >> 3), ((M >> 2) & 1), (M & 3)>(c);
    } else {
      constexpr int cc = M - 16;
      if constexpr (M >= 24) {
        c.rs0[1] = fadd_v(c.rs0[1], c.S[Xp][0][1][2 * (M - 24)]); c.rs1[1] = fadd_v(c.rs1[1], c.S[Xp][0][1][2 * (M - 24) + 1]);
        GAP();
      }
      pack1<Xp, (cc >> 3), 2 + ((cc >> 2) & 1), (cc & 3)>(c);
      if constexpr (M >= 24) GAP();
      rowsum1<Xp, 16 * (cc >> 3) + 8 + 4 * ((cc >> 2) & 1) + (cc & 3)>(c);
    }
  }
  GAP();
}
#define W4_VLO "+v"(c.vlo[0][0]), "+v"(c.vlo[0][1]), "+v"(c.vlo[0][2]), "+v"(c.vlo[0][3]), "+v"(c.vlo[1][0]), "+v"(c.vlo[1][1]), "+v"(c.vlo[1][2]), "+v"(c.vlo[1][3]), \
               "+v"(c.vlo[2][0]), "+v"(c.vlo[2][1]), "+v"(c.vlo[2][2]), "+v"(c.vlo[2][3]), "+v"(c.vlo[3][0]), "+v"(c.vlo[3][1]), "+v"(c.vlo[3][2]), "+v"(c.vlo[3][3])
#define W4_VUP "+v"(c.vup[0][0]), "+v"(c.vup[0][1]), "+v"(c.vup[0][2]), "+v"(c.vup[0][3]), "+v"(c.vup[1][0]), "+v"(c.vup[1][1]), "+v"(c.vup[1][2]), "+v"(c.vup[1][3]), \
               "+v"(c.vup[2][0]), "+v"(c.vup[2][1]), "+v"(c.vup[2][2]), "+v"(c.vup[2][3]), "+v"(c.vup[3][0]), "+v"(c.vup[3][1]), "+v"(c.vup[3][2]), "+v"(c.vup[3][3])
template <int X, bool FIRST, int... Ms> DI void phaseA_(W4C& c, const int t, iseq<Ms...>) {
  const unsigned vslot = (unsigned)((t - 1) & 1) * 0x4000u;
  (gapA<X, FIRST, Ms>(c, vslot, t), ...);
  if constexpr (FIRST) asm volatile("s_waitcnt vmcnt(0) lgkmcnt(0)\n\ts_barrier" ::: "memory");
  else { asm volatile("s_waitcnt vmcnt(0) lgkmcnt(0)" : W4_VLO :: "memory"); asm volatile("s_barrier" : W4_VUP :: "memory"); }
  GAP();
  if constexpr (!FIRST) {
    #pragma unroll
    for (int ks = 0; ks < 4; ++ks)
      #pragma unroll
      for (int db = 0; db < 4; ++db) c.Vf[db][ks] = __builtin_shufflevector(c.vlo[db][ks], c.vup[db][ks], 0, 1, 2, 3, 4, 5, 6, 7);
  }
}
template <int X, bool FIRST> DI void phaseA(W4C& c, const int t) { phaseA_<X, FIRST>(c, t, make_iseq<32>{}); }
template <int R> DI void o_scale1(float f0, float f1) {
  float t; asm volatile("v_accvgpr_read_b32 %0, a[%c2]\n\tv_mul_f32 %0, %1, %0\n\tv_accvgpr_write_b32 a[%c2], %0" : "=&v"(t) : "v"(((R >> 4) & 1) ? f1 : f0), "i"(R) : ACLOB_LO);
}
template <int... Rs> DI void o_scale(float f0, float f1, iseq<Rs...>) { (o_scale1<Rs>(f0, f1), ...); }
template <int X, bool PEEL, bool ZERO1, bool DK, bool DV, bool KR, int M> DI void gapB(W4C& c, const unsigned kslot_rd) {
  if constexpr (!PEEL) {
    if constexpr ((M < 4 && DK) || (M >= 4 && M < 8 && DV)) set_m0(c.pm0[M < 8 ? M : 0]);
    if constexpr (M == 9 && DK) set_m0(c.pfm0);
    pv_mfma<M, ZERO1>(c); GAP();
    if constexpr (M < 4 && DK)                dma16_m0(c.vksrc[M & 1], c.srdK, c.psf[M]);
    else if constexpr (M >= 4 && M < 8 && DV) dma16_m0(c.vvsrc, c.srdV, c.psf[M < 8 ? M : 0]);
    if constexpr (M == 9 && DK) { dma16_m0(c.pfvo, c.srdP, c.pfso); c.pfso += 1024u; }
  } else {
    if constexpr (M < 4 && DK)                dma16(c.pm0[M], c.vksrc[M & 1], c.srdK, c.psf[M]);
    else if constexpr (M >= 4 && M < 8 && DV) dma16(c.pm0[M], c.vvsrc, c.srdV, c.psf[M < 8 ? M : 0]);
    if constexpr (M == 9 && DK) { dma16(c.pfm0, c.pfvo, c.srdP, c.pfso); c.pfso += 1024u; }
  }
  if constexpr (M >= 1 && M <= 16 && KR) k_read<M - 1>(c, kslot_rd);
  if constexpr (M >= 8 && M < 16) {
    constexpr int i = M - 8, q = i >> 2, r = 4 * (i & 3);
    if constexpr (r == 0)      { c.pmx[q][0] = max3v(c.S[X][0][q][0], c.S[X][0][q][1], c.S[X][0][q][2]); c.pmx[q][1] = max3v(c.S[X][0][q][3], c.S[X][1][q][0], c.S[X][1][q][1]); c.pmx[q][2] = max2v(c.S[X][1][q][2], c.S[X][1][q][3]); }
    else { c.pmx[q][0] = max3v(c.pmx[q][0], c.S[X][0][q][r], c.S[X][0][q][r + 1]); c.pmx[q][1] = max3v(c.pmx[q][1], c.S[X][0][q][r + 2], c.S[X][0][q][r + 3]); c.pmx[q][2] = max3v(c.pmx[q][2], c.S[X][1][q][r], c.S[X][1][q][r + 1]);
      if constexpr (r == 4) c.pmx[q][3] = max2v(c.S[X][1][q][r + 2], c.S[X][1][q][r + 3]); else c.pmx[q][3] = max3v(c.pmx[q][3], c.S[X][1][q][r + 2], c.S[X][1][q][r + 3]); }
  }
  if constexpr (M == 16) { c.pmx[0][0] = max3v(c.pmx[0][0], c.pmx[0][1], c.pmx[0][2]); c.pmx[1][0] = max3v(c.pmx[1][0], c.pmx[1][1], c.pmx[1][2]); }
  if constexpr (M == 17) {
    const float m0 = max2v(c.pmx[0][0], c.pmx[0][3]), m1 = max2v(c.pmx[1][0], c.pmx[1][3]);
    auto p0 = __builtin_amdgcn_permlane32_swap(__float_as_uint(m0), __float_as_uint(m0), false, false);
    auto p1 = __builtin_amdgcn_permlane32_swap(__float_as_uint(m1), __float_as_uint(m1), false, false);
    c.rmax[0] = max2f(__uint_as_float(p0[0]), __uint_as_float(p0[1])); c.rmax[1] = max2f(__uint_as_float(p1[0]), __uint_as_float(p1[1]));
  }
  if constexpr (M == 18) {
    if constexpr (PEEL) {
      #pragma unroll
      for (int q = 0; q < 2; ++q) { c.mhat[q] = c.rmax[q]; c.lsum[q] = 0.f; c.fct[q] = 1.f;
        #pragma unroll
        for (int kb = 0; kb < 2; ++kb) { _Pragma("unroll") for (int e = 0; e < 16; ++e) c.S[X][kb][q][e] = fsub_s(c.S[X][kb][q][e], c.rmax[q]); }
        negm_bcast(c.NEGM[q], w4_aone(), -c.mhat[q]);
      }
    } else {
      const bool grew = (c.rmax[0] > THR) | (c.rmax[1] > THR);
      if (__builtin_expect(__any((int)grew), 0)) {
        #pragma unroll
        for (int q = 0; q < 2; ++q) { const float dl = max2f(c.rmax[q], 0.f); c.delta[q] = dl; c.mhat[q] = fadd_s(c.mhat[q], dl); c.fct[q] = __builtin_amdgcn_exp2f(-dl);
          asm volatile("s_nop 1" : "+v"(c.fct[q])); c.rs0[q] = fmul_s(c.rs0[q], c.fct[q]); c.rs1[q] = fmul_s(c.rs1[q], c.fct[q]);
          #pragma unroll
          for (int kb = 0; kb < 2; ++kb) { _Pragma("unroll") for (int e = 0; e < 16; ++e) c.S[X][kb][q][e] = fsub_s(c.S[X][kb][q][e], dl); }
          negm_bcast(c.NEGM[q], w4_aone(), -c.mhat[q]);
        }
        c.pend = true;
      }
    }
  }
  if constexpr (M == 18 && PEEL) { c.rs0[0] = c.rs0[1] = c.rs1[0] = c.rs1[1] = 0.f; }
  if constexpr (M >= 19) {
    constexpr int g = M - 19, e_lo = (32 * g) / 13, e_hi = (32 * (g + 1)) / 13;
    {
      constexpr int p_lo = g ? (32 * (g - 1)) / 13 : 0, p_hi = g ? (32 * g) / 13 : 0;
      if constexpr (p_lo + 0 < p_hi && p_lo + 0 < 16) { constexpr int e = p_lo + 0; if constexpr (e & 1) c.rs1[e >> 4] = fadd_v(c.rs1[e >> 4], c.S[X][0][e >> 4][e & 15]); else c.rs0[e >> 4] = fadd_v(c.rs0[e >> 4], c.S[X][0][e >> 4][e & 15]); }
      if constexpr (p_lo + 1 < p_hi && p_lo + 1 < 16) { constexpr int e = p_lo + 1; if constexpr (e & 1) c.rs1[e >> 4] = fadd_v(c.rs1[e >> 4], c.S[X][0][e >> 4][e & 15]); else c.rs0[e >> 4] = fadd_v(c.rs0[e >> 4], c.S[X][0][e >> 4][e & 15]); }
    }
    GAP();
    if constexpr (e_lo + 0 < e_hi) c.S[X][0][(e_lo + 0) >> 4][(e_lo + 0) & 15] = __builtin_amdgcn_exp2f(c.S[X][0][(e_lo + 0) >> 4][(e_lo + 0) & 15]);
    if constexpr (e_lo + 1 < e_hi) c.S[X][0][(e_lo + 1) >> 4][(e_lo + 1) & 15] = __builtin_amdgcn_exp2f(c.S[X][0][(e_lo + 1) >> 4][(e_lo + 1) & 15]);
    if constexpr (e_lo + 2 < e_hi) c.S[X][0][(e_lo + 2) >> 4][(e_lo + 2) & 15] = __builtin_amdgcn_exp2f(c.S[X][0][(e_lo + 2) >> 4][(e_lo + 2) & 15]);
    GAP();
    { constexpr int p_lo = g ? (32 * (g - 1)) / 13 : 0, p_hi = g ? (32 * g) / 13 : 0;
      if constexpr (p_lo + 2 < p_hi && p_lo + 2 < 16) { constexpr int e = p_lo + 2; if constexpr (e & 1) c.rs1[e >> 4] = fadd_v(c.rs1[e >> 4], c.S[X][0][e >> 4][e & 15]); else c.rs0[e >> 4] = fadd_v(c.rs0[e >> 4], c.S[X][0][e >> 4][e & 15]); }
    }
  }
  if constexpr (M == 31) asm volatile("" :: "v"(c.S[X][0][0]), "v"(c.S[X][0][1]), "v"(c.NEGM[0]), "v"(c.NEGM[1]), "v"(c.rs0[0]), "v"(c.rs0[1]), "v"(c.rs1[0]), "v"(c.rs1[1]));
  GAP();
}
template <int X, bool PEEL, bool ZERO1, bool DK, bool DV, bool KR, int... Ms> DI void phaseB_(W4C& c, const int t, iseq<Ms...>) {
  const unsigned kslot_rd = (unsigned)((t + 1) & 1) * 0x4000u;
  if constexpr (!PEEL) asm volatile("s_nop 1" ::: "memory");
  GAP();
  (gapB<X, PEEL, ZERO1, DK, DV, KR, Ms>(c, kslot_rd), ...);
  {
  }
  if constexpr (!PEEL) {
    if (__builtin_expect(c.pend, 0)) {
      asm volatile("s_nop 7\n\ts_nop 7" ::: "memory");
      o_scale(c.fct[0], c.fct[1], make_iseq<128>{});
      asm volatile("s_nop 1" ::: "memory");
      c.pend = false;
    }
  }
  asm volatile("s_waitcnt lgkmcnt(0)" ::: "memory");
  GAP();
}
template <int X, bool PEEL, bool ZERO1, bool DK = true, bool DV = true, bool KR = true> DI void phaseB(W4C& c, const int t) { phaseB_<X, PEEL, ZERO1, DK, DV, KR>(c, t, make_iseq<32>{}); }
template <int... Is> DI void k_reads(W4C& c, unsigned slot, iseq<Is...>) { (k_read<Is>(c, slot), ...); }
template <int... Is> DI void tr_reads(W4C& c, unsigned vslot, iseq<Is...>) { (tr_read<Is>(c, vslot), ...); }
template <int X, int... Cs> DI void packs(W4C& c, iseq<Cs...>) { (pack1<X, ((Cs >> 3) & 1), 2 * (Cs >> 4) + ((Cs >> 2) & 1), (Cs & 3)>(c), ...); }
template <int X, int J> DI void rowsum1_padded(W4C& c) { rowsum1<X, J>(c); asm volatile("s_nop 1"); }
template <int X, int... Js> DI void rowsums(W4C& c, iseq<Js...>) { (rowsum1_padded<X, Js>(c), ...); }
template <int... Ms> DI void pv_all(W4C& c, iseq<Ms...>) { (pv_mfma<Ms, false>(c), ...); }
#define O_AUX 16
template <int F> DI void o_store(float il, srd_t srdO, unsigned voff, unsigned soff) {
  constexpr int qb = F >> 3, st = F & 7, src = 16 * (2 * (st >> 1) + qb) + 8 * (st & 1);
  u32x4 w;
  w[0] = cvtpk(acc_rd<src + 0>() * il, acc_rd<src + 1>() * il); w[1] = cvtpk(acc_rd<src + 2>() * il, acc_rd<src + 3>() * il);
  w[2] = cvtpk(acc_rd<src + 4>() * il, acc_rd<src + 5>() * il); w[3] = cvtpk(acc_rd<src + 6>() * il, acc_rd<src + 7>() * il);
  __builtin_amdgcn_raw_buffer_store_b128(w, srdO, voff, soff + (unsigned)(qb * 256 + st) * 1024u, O_AUX);
}
template <int... Fs> DI void o_stores(float il0, float il1, srd_t srdO, unsigned voff, unsigned soff, iseq<Fs...>) { (o_store<Fs>((Fs >> 3) ? il1 : il0, srdO, voff, soff), ...); }

__global__ __launch_bounds__(256, 1)
void k2_main(const unsigned short* __restrict__ Kc, const unsigned short* __restrict__ Vc, const unsigned short* __restrict__ Wq,
             const unsigned short* __restrict__ Wg, const unsigned short* __restrict__ Wu,
             const unsigned short* __restrict__ xn, unsigned short* __restrict__ hbuf, unsigned short* __restrict__ obuf,
             const unsigned short* __restrict__ Wo, const unsigned short* __restrict__ Wd) {
  extern __shared__ __attribute__((aligned(16))) char lds[];
  const unsigned tid = threadIdx.x, lane = tid & 63u, l32 = lane & 31u, hi = lane >> 5;
  const unsigned wid = rfl(tid >> 6);
  const unsigned ldsb = (unsigned)(size_t)lds;
  const unsigned bx = blockIdx.x, idx = bx >> 3, j = (bx & 7u) * 4u + (idx & 3u), rb = idx >> 2;
  const unsigned tb0 = rb * 8u + 2u * wid;
  float lsum0, lsum1;
  ST_DECL; STR(8); ST(0);
  {
    GC g;
    const unsigned q4 = (lane >> 2) & 3u, p4 = lane & 3u, blk = (lane >> 4) & 1u, isU = wid >> 1, wl = wid & 1u;
    const unsigned rowu = (4u * hi + q4) ^ (4u * blk);
    g.trb[0] = ldsb + WRING + rowu * 32u + p4 * 8u + blk * 8192u;
    g.trb[1] = ldsb + WRING + rowu * 32u + p4 * 8u + blk * 10240u;
    g.trb[2] = ldsb + WRING + (blk << 5) + ((p4 & 1u) << 4) + ((p4 >> 1) << 3) + (q4 << 6) + (hi << 8);
    const unsigned rowx = ((lane >> 1) & 7u) ^ (4u * isU);
    g.wsrc1 = rowx * (FF * 2u) + (lane >> 4) * 32u + (lane & 1u) * 16u;
    #pragma unroll
    for (int p = 0; p < 5; ++p) { const unsigned u = 4u * p + (lane >> 4), rg = u / 5u, ct = u - 5u * rg; g.wsrc2[p] = (8u * rg + rowx) * (FF * 2u) + ct * 32u + (lane & 1u) * 16u; }
    g.wsrc3 = 8192u * ((lane >> 2) & 7u) + 64u * hi + 16u * (lane & 3u);
    g.xvo[0] = lane * 16u; g.xvo[1] = lane * 16u + 36864u; g.hvo = lane * 16u;
    g.srdX = mk_srd((const char*)xn + (size_t)tb0 * 36864u, 2u * 36864u);
    g.srdGU = mk_srd(isU ? Wu : Wg, DM * FF * 2u);
    g.srdWq = mk_srd(Wq, DM * NH * DH * 2u);
    g.srdH = mk_srd((char*)hbuf + (size_t)tb0 * 288u * 1024u, 2u * 288u * 1024u);
    g.srdK = mk_srd((const char*)Kc + (size_t)j * SEQ * 256u, SEQ * 256u);
    g.srdV = mk_srd((const char*)Vc + (size_t)j * SEQ * 256u, SEQ * 256u);
    g.m0g1 = ldsb + WRING + isU * 8192u + wl * 4096u;   g.sof1 = wl * 32u * (FF * 2u) + j * 288u;
    g.m0g2 = ldsb + WRING + isU * 10240u + wl * 5120u;  g.sof2 = wl * 32u * (FF * 2u) + j * 288u + 128u;
    g.m0q = ldsb + WRING + wid * 4096u;                 g.sofq = wid * 16u * 8192u + j * 256u;
    g.hso = j * 9u * 1024u;
    g.pfq = (bx * 4u + wid) * 5u * 1024u; g.pfm = ldsb + KRING + wid * 1024u;
    #pragma unroll
    for (int par = 0; par < 2; ++par) g.vksrc[par] = KRS * (lane >> 4) + 16u * ((lane & 15u) ^ (4u * par + (lane >> 4)));
    g.vvsrc = KRS * ((lane >> 2) & 7u) + 64u * hi + 16u * (lane & 3u);
    g.m0k = ldsb + KRING + 4096u * wid; g.m0v = ldsb + VRING + 4096u * wid; g.sofk = KRS * 16u * wid; g.sofv = KRS * 16u * wid;
    g.kc0 = -2.302208198f; g.kc1 = -0.1029432397f;
    asm volatile("" : "+v"(g.kc0), "+v"(g.kc1));
    asm volatile("s_nop 4" ::: "memory");
    g_wdma_all0(g, make_iseq<4>{}); g_xld_all0(g, make_iseq<8>{}); g_wdma_all1(g, make_iseq<4>{});
    { GC& c = g; asm volatile("s_waitcnt vmcnt(4)\n\ts_barrier" : G_B8(0) :: "memory"); }
    g_tr_all0(g, make_iseq<8>{});
    g_land<4, 0>(g);
    ST(1);
    g_chunks<0>(g, make_iseq<9>{});
    ST(2);
    g_chunks<9>(g, make_iseq<9>{});
    ST(3);
    g_chunks<18>(g, make_iseq<9>{});
    ST(4);
    asm volatile("s_nop 7\n\ts_nop 7" ::: "memory");
    q_packs(0.12751743082f, make_iseq<64>{});
  }
  W4C c;
  #pragma unroll
  for (int jj = 0; jj < 4; ++jj) c.vkb[jj] = ldsb + KRING + 256u * l32 + 16u * ((2u * jj + hi) ^ (l32 & 7u));
  c.vvb = ldsb + VRING + (((lane & 3u) << 3) | (((lane >> 2) & 3u) << 6) | (((lane >> 4) & 1u) << 5) | (hi << 8));
  #pragma unroll
  for (int par = 0; par < 2; ++par) c.vksrc[par] = KRS * (lane >> 4) + 16u * ((lane & 15u) ^ (4u * par + (lane >> 4)));
  c.vvsrc = KRS * ((lane >> 2) & 7u) + 64u * hi + 16u * (lane & 3u);
  c.m0k = ldsb + KRING + 4096u * wid; c.m0v = ldsb + VRING + 4096u * wid; c.sofk = KRS * 16u * wid; c.sofv = KRS * 16u * wid;
  c.srdK = mk_srd((const char*)Kc + (size_t)j * SEQ * 256u, SEQ * 256u);
  c.srdV = mk_srd((const char*)Vc + (size_t)j * SEQ * 256u, SEQ * 256u);
  {
    const unsigned g = bx * 4u + wid, isD = g >> 9, npc = isD ? 11u : 9u, p0 = (g & 511u) * npc * 1024u, tot = isD ? FF * DM * 2u : NH * DH * DM * 2u;
    c.srdP = mk_srd(isD ? Wd : Wo, (p0 + npc * 1024u < tot) ? p0 + npc * 1024u : tot);
    c.pfso = p0; c.pfvo = lane * 16u; c.pfm0 = ldsb + WRING + 1024u * wid;
  }
  asm volatile("s_waitcnt vmcnt(0)\n\ts_barrier" ::: "memory");
  k_reads(c, 0u, make_iseq<16>{});
  asm volatile("s_waitcnt lgkmcnt(0)\n\ts_nop 1" ::: "memory");
  ST(5);
  c.mhat[0] = c.mhat[1] = 0.f; c.lsum[0] = c.lsum[1] = 0.f; c.fct[0] = c.fct[1] = 1.f; c.pend = false;
  constexpr int T = NT_KV;
  phaseA<0, true>(c, 0);  phaseB<0, true,  false>(c, 0);
  phaseA<1, false>(c, 1); phaseB<1, false, true>(c, 1);
  for (int t = 2; t < T - 2; t += 2) {
    phaseA<0, false>(c, t);     phaseB<0, false, false>(c, t);
    phaseA<1, false>(c, t + 1); phaseB<1, false, false>(c, t + 1);
  }
  phaseA<0, false>(c, T - 2); phaseB<0, false, false, false, true, true>(c, T - 2);
  phaseA<1, false>(c, T - 1); phaseB<1, false, false, false, false, false>(c, T - 1);
  {
    const unsigned vslot = (unsigned)((T - 1) & 1) * 0x4000u;
    tr_reads(c, vslot, make_iseq<32>{});
    #pragma unroll
    for (int e = 0; e < 32; ++e) c.S[1][1][e >> 4][e & 15] = __builtin_amdgcn_exp2f(c.S[1][1][e >> 4][e & 15]);
    packs<1>(c, make_iseq<32>{});
    #pragma unroll
    for (int e = 0; e < 16; e += 2) { c.rs0[1] = fadd_v(c.rs0[1], c.S[1][0][1][e]); c.rs1[1] = fadd_v(c.rs1[1], c.S[1][0][1][e + 1]); asm volatile("s_nop 1"); }
    rowsums<1>(c, iseq<8, 9, 10, 11, 12, 13, 14, 15, 24, 25, 26, 27, 28, 29, 30, 31>{});
    #pragma unroll
    for (int qb = 0; qb < 2; ++qb) { const float rs = fadd_s(c.rs0[qb], c.rs1[qb]);
      auto pr = __builtin_amdgcn_permlane32_swap(__float_as_uint(rs), __float_as_uint(rs), false, false);
      c.lsum[qb] = fadd_s(c.lsum[qb], fadd_s(__uint_as_float(pr[0]), __uint_as_float(pr[1]))); }
    asm volatile("s_waitcnt lgkmcnt(0)" : W4_VLO :: "memory"); asm volatile("" : W4_VUP :: "memory");
    GAP();
    #pragma unroll
    for (int ks = 0; ks < 4; ++ks)
      #pragma unroll
      for (int db = 0; db < 4; ++db) c.Vf[db][ks] = __builtin_shufflevector(c.vlo[db][ks], c.vup[db][ks], 0, 1, 2, 3, 4, 5, 6, 7);
    asm volatile("s_nop 1" ::: "memory");
    pv_all(c, make_iseq<32>{});
    asm volatile("s_nop 7\n\ts_nop 7" ::: "memory");
  }
  lsum0 = c.lsum[0]; lsum1 = c.lsum[1];
  ST(6);
  {
    const srd_t srdO = mk_srd((char*)obuf + (size_t)tb0 * 256u * 1024u, 2u * 256u * 1024u);
    o_stores(__builtin_amdgcn_rcpf(lsum0), __builtin_amdgcn_rcpf(lsum1), srdO, lane * 16u, j * 8u * 1024u, make_iseq<16>{});
  }
  ST(7); STR(9);
  ST_OUT((char*)xn + (80u << 20));
}

#define OUT_AUX 16
constexpr unsigned K3SLOT = 36864u;
constexpr size_t SHM_K3 = 4 * 36864;
typedef __attribute__((address_space(1))) unsigned gu32;
struct K3C {
  unsigned trb, wsrc[9], bvo[2];
  srd_t srdW, srdA;
  unsigned m0w, sofw;
  bf16x8 B[4][4][2];
  bf16x4 alo[2][9], aup[2][9];
  bf16x8 A[2][9];
};

template <int ST, int CT, int HALF> DI void k3_tr(K3C& c, unsigned base) {
  constexpr int off = ((4 * ST + HALF) * 9 + CT) * 256, par = ST & 1;
  if constexpr (HALF == 0) asm volatile("ds_read_b64_tr_b16 %0, %1 offset:%c2" : "=v"(c.alo[par][CT]) : "v"(base), "i"(off) : "memory");
  else                     asm volatile("ds_read_b64_tr_b16 %0, %1 offset:%c2" : "=v"(c.aup[par][CT]) : "v"(base), "i"(off) : "memory");
}
template <int BUF, int X> DI void k3_bld(K3C& c, unsigned soff) {
  constexpr int st = X >> 1, tg = X & 1;
  asm volatile("buffer_load_dwordx4 %0, %1, %2, %3 offen offset:%c4" : "=v"(c.B[BUF][st][tg]) : "v"(c.bvo[tg]), "s"(c.srdA), "s"(soff + (st >> 1) * 4096u), "i"((st & 1) * 2048) : "memory");
}
template <int BUF, int ST, int M> DI void k3_gap(K3C& c, unsigned trcur, unsigned trnext, unsigned bsoff, unsigned wm0, unsigned wsoff) {
  constexpr int ct = M >> 1, tg = M & 1, par = ST & 1;
  if constexpr (tg == 0) {
    asm volatile("s_waitcnt lgkmcnt(15)" : "+v"(c.alo[par][ct]), "+v"(c.aup[par][ct]) :: "memory");
    c.A[par][ct] = __builtin_shufflevector(c.alo[par][ct], c.aup[par][ct], 0, 1, 2, 3, 4, 5, 6, 7);
  }
  asm volatile("v_mfma_f32_16x16x32_bf16 a[%c2:%c3], %0, %1, a[%c2:%c3]" :: "v"(c.A[par][ct]), "v"(c.B[BUF][ST][tg]), "i"(4 * M), "i"(4 * M + 3) : ACLOB_LO);
  k3_tr<(ST + 1) & 3, ct, tg>(c, ST == 3 ? trnext : trcur);
  { constexpr int G = ST * 18 + M;
    if constexpr (G < 48 && G % 6 == 2) k3_bld<(BUF + 3) & 3, G / 6>(c, bsoff);
    if constexpr (G >= 55 && (G & 1) == 1) dma16(wm0 + 1024u * ((G - 55) >> 1), c.wsrc[(G - 55) >> 1], c.srdW, wsoff); }
  GAP();
}
template <int BUF, int ST, int... Ms> DI void k3_step(K3C& c, unsigned trcur, unsigned trnext, unsigned bsoff, unsigned wm0, unsigned wsoff, iseq<Ms...>) { (k3_gap<BUF, ST, Ms>(c, trcur, trnext, bsoff, wm0, wsoff), ...); }
template <int BUF> DI void k3_chunk(K3C& c, unsigned trcur, unsigned trnext, unsigned bsoff, unsigned wm0, unsigned wsoff) {
  k3_step<BUF, 0>(c, trcur, trnext, bsoff, wm0, wsoff, make_iseq<18>{});
  k3_step<BUF, 1>(c, trcur, trnext, bsoff, wm0, wsoff, make_iseq<18>{});
  k3_step<BUF, 2>(c, trcur, trnext, bsoff, wm0, wsoff, make_iseq<18>{});
  asm volatile("s_waitcnt vmcnt(25)\n\ts_barrier" : G_B8((BUF + 1) & 3) :: "memory"); GAP();
  k3_step<BUF, 3>(c, trcur, trnext, bsoff, wm0, wsoff, make_iseq<18>{});
}
template <int... Rs> DI void k3_zero(iseq<Rs...>) { (acc_wr<Rs>(0u), ...); }
template <int I> DI f32x4 k3_acc() { f32x4 r = {acc_rd<4 * I>(), acc_rd<4 * I + 1>(), acc_rd<4 * I + 2>(), acc_rd<4 * I + 3>()}; return r; }
template <int... Is> DI void k3_accs(f32x4 (&a)[18], iseq<Is...>) { ((a[Is] = k3_acc<Is>()), ...); }
template <int BUF, int... Xs> DI void k3_bld_all(K3C& c, unsigned soff, iseq<Xs...>) { (k3_bld<BUF, Xs>(c, soff), ...); }
template <int... Xs> DI void k3_tr_all(K3C& c, unsigned base, iseq<Xs...>) { (k3_tr<0, (Xs >> 1), (Xs & 1)>(c, base), ...); }

__global__ __launch_bounds__(256, 1)
void k3_out(const unsigned short* __restrict__ x, const unsigned short* __restrict__ Wo, const unsigned short* __restrict__ Wd,
            const unsigned short* __restrict__ obuf, const unsigned short* __restrict__ hbuf,
            float* __restrict__ slab, unsigned* __restrict__ flags, unsigned short* __restrict__ out) {
  extern __shared__ __attribute__((aligned(16))) char lds[];
  const unsigned tid = threadIdx.x, lane = tid & 63u;
  const unsigned wid = rfl(tid >> 6);
  const unsigned ldsb = (unsigned)(size_t)lds;
  const unsigned bx = blockIdx.x, xcd = bx & 7u, idx = bx >> 3, role = xcd >> 1, mt = (xcd & 1u) * 8u + (idx >> 2), nt = idx & 3u;
  const bool isF = role >= 2u;
  const unsigned nchunk = isF ? 18u : 16u, steps_row = isF ? 288u : 256u;
  const unsigned k0 = (role & 1u) * nchunk * 128u;
  const unsigned tb = mt * 4u + wid;
  ST_DECL; STR(8); ST(0);
  K3C c;
  const unsigned kg = lane >> 4, q4 = (lane >> 2) & 3u, p4 = lane & 3u;
  #pragma unroll
  for (int tg = 0; tg < 2; ++tg) c.bvo[tg] = (kg >> 1) * 1024u + (kg & 1u) * 512u + (16u * tg + (lane & 15u)) * 16u;
  const unsigned kend = k0 + nchunk * 128u;
  c.srdA = mk_srd((const char*)(isF ? hbuf : obuf) + (size_t)tb * steps_row * 1024u, (kend >> 4) * 1024u);
  const unsigned bs0 = (k0 >> 4) * 1024u;
  asm volatile("s_nop 4" ::: "memory");
  k3_bld_all<0>(c, bs0, make_iseq<8>{});
  GAP();
  c.srdW = mk_srd(isF ? Wd : Wo, kend * (DM * 2u));
  c.trb = ldsb + (kg >> 1) * (2u * 9u * 256u) + (4u * (kg & 1u) + q4) * 32u + p4 * 8u;
  #pragma unroll
  for (int p = 0; p < 9; ++p) { const unsigned u = 4u * p + kg, rg = u / 9u, ct = u - 9u * rg; c.wsrc[p] = (8u * rg + ((lane >> 1) & 7u)) * (DM * 2u) + ct * 32u + (lane & 1u) * 16u; }
  c.m0w = ldsb + wid * 9216u;
  c.sofw = (k0 + wid * 32u) * (DM * 2u) + nt * 288u;
  asm volatile("s_nop 4" ::: "memory");
  #pragma unroll
  for (int p = 0; p < 9; ++p) dma16(c.m0w + 1024u * p, c.wsrc[p], c.srdW, c.sofw);
  k3_bld_all<1>(c, bs0 + 8192u, make_iseq<8>{});
  #pragma unroll
  for (int p = 0; p < 9; ++p) dma16(c.m0w + K3SLOT + 1024u * p, c.wsrc[p], c.srdW, c.sofw + 128u * (DM * 2u));
  k3_bld_all<2>(c, bs0 + 16384u, make_iseq<8>{});
  #pragma unroll
  for (int p = 0; p < 9; ++p) dma16(c.m0w + 2u * K3SLOT + 1024u * p, c.wsrc[p], c.srdW, c.sofw + 256u * (DM * 2u));
  k3_zero(make_iseq<72>{});
  asm volatile("s_waitcnt vmcnt(34)\n\ts_barrier" : G_B8(0) :: "memory");
  k3_tr_all(c, c.trb, make_iseq<18>{});
  asm volatile("s_waitcnt lgkmcnt(0)" ::: "memory"); GAP();
  ST(1);
#define K3_CHUNK(BUF, ch) k3_chunk<BUF>(c, c.trb + (BUF) * K3SLOT, c.trb + (((BUF) + 1) & 3) * K3SLOT, bs0 + ((ch) + 3u) * 8192u, c.m0w + (((BUF) + 3) & 3) * K3SLOT, c.sofw + ((ch) + 3u) * 128u * (DM * 2u))
  for (unsigned ch = 0;; ch += 4u) {
    K3_CHUNK(0, ch); K3_CHUNK(1, ch + 1u);
    if (ch + 2u == nchunk) break;
    K3_CHUNK(2, ch + 2u); K3_CHUNK(3, ch + 3u);
    if (ch + 4u == nchunk) break;
  }
  asm volatile("s_waitcnt vmcnt(0) lgkmcnt(0)\n\ts_nop 7\n\ts_nop 7" : G_B8(0), G_B8(1) :: "memory");
  asm volatile("" : G_B8(2), G_B8(3) :: "memory");
  ST(2);
  const unsigned tile = mt * 4u + nt;
  f32x4 acc[18]; k3_accs(acc, make_iseq<18>{});
  u32x2 xr[5];
  const unsigned tok0 = mt * 128u + wid * 32u + (lane & 15u), col0 = nt * 144u + 4u * (lane >> 4);
  #pragma unroll
  for (int n = 0; n < 5; ++n) { const unsigned i = (role + 4u * n < 18u) ? role + 4u * n : role; xr[n] = *(const u32x2*)(x + (size_t)(tok0 + 16u * (i & 1u)) * DM + col0 + 16u * (i >> 1)); }
  const srd_t srdS = mk_srd(slab, 64u * 16u * 10240u);
  const unsigned mybase = ((tile * 4u + role) * 4u + wid) * 10240u;
  #pragma unroll
  for (int r = 0; r < 4; ++r)
    if ((unsigned)r != role) {
      const u32x4 w0 = {cvtpk(acc[r][0], acc[r][1]), cvtpk(acc[r][2], acc[r][3]), cvtpk(acc[r + 4][0], acc[r + 4][1]), cvtpk(acc[r + 4][2], acc[r + 4][3])};
      const u32x4 w1 = {cvtpk(acc[r + 8][0], acc[r + 8][1]), cvtpk(acc[r + 8][2], acc[r + 8][3]), cvtpk(acc[r + 12][0], acc[r + 12][1]), cvtpk(acc[r + 12][2], acc[r + 12][3])};
      __builtin_amdgcn_raw_buffer_store_b128(w0, srdS, lane * 16u, mybase + r * 2560u, 16);
      __builtin_amdgcn_raw_buffer_store_b128(w1, srdS, lane * 16u, mybase + r * 2560u + 1024u, 16);
      if (r < 2) { const u32x2 w2 = {cvtpk(acc[(r & 1) + 16][0], acc[(r & 1) + 16][1]), cvtpk(acc[(r & 1) + 16][2], acc[(r & 1) + 16][3])}; __builtin_amdgcn_raw_buffer_store_b64(w2, srdS, lane * 8u, mybase + r * 2560u + 2048u, 16); }
    }
  asm volatile("s_waitcnt vmcnt(0)" ::: "memory");
  __syncthreads();
  ST(3);
  gu32* fl = (gu32*)flags;
  if (tid == 0) __hip_atomic_store(fl + tile * 4u + role, 1u, __ATOMIC_RELAXED, __HIP_MEMORY_SCOPE_AGENT);
  if (wid == 0) {
    gu32* w = fl + tile * 4u + (lane & 3u);
    while (!__all((int)(__hip_atomic_load(w, __ATOMIC_RELAXED, __HIP_MEMORY_SCOPE_AGENT) == 1u || (lane & 3u) == role))) __builtin_amdgcn_s_sleep(1);
  }
  asm volatile("" ::: "memory");
  __syncthreads();
  ST(4);
  u32x4 pa[3], pb[3]; u32x2 pc[3];
  #pragma unroll
  for (unsigned q = 0; q < 3; ++q) {
    const unsigned src = ((tile * 4u + ((role + 1u + q) & 3u)) * 4u + wid) * 10240u + role * 2560u;
    pa[q] = __builtin_amdgcn_raw_buffer_load_b128(srdS, lane * 16u, src, 16);
    pb[q] = __builtin_amdgcn_raw_buffer_load_b128(srdS, lane * 16u, src + 1024u, 16);
    pc[q] = u32x2{0u, 0u};
    if (role < 2u) pc[q] = __builtin_amdgcn_raw_buffer_load_b64(srdS, lane * 8u, src + 2048u, 16);
  }
  #pragma unroll
  for (int n = 0; n < 5; ++n) {
    const unsigned i = role + 4u * n;
    if (i < 18u) {
      f32x4 own = acc[0];
      #pragma unroll
      for (int ii = 0; ii < 18; ++ii) if ((ii >> 2) == n && (unsigned)(ii & 3) == role) own = acc[ii];
      f32x4 pq[3];
      #pragma unroll
      for (int q = 0; q < 3; ++q) {
        const unsigned w0 = n == 4 ? pc[q][0] : (n < 2 ? pa[q][2 * (n & 1)] : pb[q][2 * (n & 1)]), w1 = n == 4 ? pc[q][1] : (n < 2 ? pa[q][2 * (n & 1) + 1] : pb[q][2 * (n & 1) + 1]);
        pq[q] = f32x4{__uint_as_float(w0 << 16), __uint_as_float(w0 & 0xffff0000u), __uint_as_float(w1 << 16), __uint_as_float(w1 & 0xffff0000u)};
      }
      const bool odd = role & 1u;
      float o[4];
      #pragma unroll
      for (int e = 0; e < 4; ++e) {
        const float mine = own[e] + (odd ? pq[2][e] : pq[0][e]), other = pq[1][e] + (odd ? pq[0][e] : pq[2][e]);
        const float s = isF ? other : mine, f = isF ? mine : other;
        const float g = f * __builtin_amdgcn_rcpf(1.f + __builtin_amdgcn_exp2f(-1.4426950408889634f * f));
        const float xv = __uint_as_float((e & 1) ? (xr[n][e >> 1] & 0xffff0000u) : (xr[n][e >> 1] << 16));
        o[e] = xv + s * g;
      }
      u32x2 w = {cvtpk(o[0], o[1]), cvtpk(o[2], o[3])};
      __builtin_amdgcn_raw_buffer_store_b64(w, mk_srd(out, SEQ * DM * 2u), ((tok0 + 16u * (i & 1u)) * DM + col0 + 16u * (i >> 1)) * 2u, 0, OUT_AUX);
    }
  }
  ST(5); STR(9);
  ST_OUT((char*)flags + (10u << 20));
}

extern "C" void kernel_launch(void* const* d_in, const int* in_sizes, int n_in, void* d_out, int out_size, void* d_ws, size_t ws_size, hipStream_t stream) {
  const unsigned short* x  = (const unsigned short*)d_in[0];
  const unsigned short* kc = (const unsigned short*)d_in[1];
  const unsigned short* vc = (const unsigned short*)d_in[2];
  const unsigned short* wq = (const unsigned short*)d_in[3];
  const unsigned short* wo = (const unsigned short*)d_in[4];
  const unsigned short* wg = (const unsigned short*)d_in[6];
  const unsigned short* wu = (const unsigned short*)d_in[7];
  const unsigned short* wd = (const unsigned short*)d_in[8];
  char* ws = (char*)d_ws;
  (void)hipFuncSetAttribute((const void*)k2_main, hipFuncAttributeMaxDynamicSharedMemorySize, (int)SHM_K2);
  (void)hipFuncSetAttribute((const void*)k3_out, hipFuncAttributeMaxDynamicSharedMemorySize, (int)SHM_K3);
  k1_layernorm<<<256, 256, 0, stream>>>(x, (unsigned short*)(ws + WS_XN), (unsigned*)(ws + WS_FLAG));
  k2_main<<<256, 256, SHM_K2, stream>>>(kc, vc, wq, wg, wu, (const unsigned short*)(ws + WS_XN), (unsigned short*)(ws + WS_H), (unsigned short*)(ws + WS_O), wo, wd);
  k3_out<<<256, 256, SHM_K3, stream>>>(x, wo, wd, (const unsigned short*)(ws + WS_O), (const unsigned short*)(ws + WS_H), (float*)(ws + WS_SLAB), (unsigned*)(ws + WS_FLAG), (unsigned short*)d_out);
}
```
